```python
import torch

def _softmax(x, axis=-1):
    return torch.softmax(x, dim=axis)
def _gelu(x):
    return torch.nn.functional.gelu(x, approximate="tanh")
def _silu(x):
    return torch.nn.functional.silu(x)
def _sigmoid(x):
    return torch.sigmoid(x)
def _layernorm(x, axis=-1, eps=1e-5):
    m = x.mean(dim=axis, keepdim=True)
    v = x.var(dim=axis, keepdim=True, unbiased=False)
    return (x - m) / torch.sqrt(v + eps)
def _scatter_add(tgt, idx, src, axis=0):
    return tgt.clone().index_add_(axis, idx, src)
def _segment_sum(src, seg, n):
    out = torch.zeros((n,) + src.shape[1:], dtype=src.dtype)
    return out.index_add_(0, seg, src)
def _pad(x, pw):
    flat = tuple(v for pair in reversed(pw) for v in pair)
    return torch.nn.functional.pad(x, flat)

kernel_name = 'sequential_preLN__ssm_conv_h3__mixer_channel_mlp__prompt'

def setup_inputs():

    _w = (lambda a: (lambda t: t.to(torch.bfloat16) if t.is_floating_point() else t)(torch.as_tensor(a)))
    def _A_setup():
        B, S, D = 1, 2048, 512
        ks = 4
        return {
            "q": _w((lambda *s: torch.randn(*s, dtype=torch.bfloat16) * 0.05)(B, S, D)),
            "k": _w((lambda *s: torch.randn(*s, dtype=torch.bfloat16) * 0.2)(B, S, D)),
            "v": _w((lambda *s: torch.randn(*s, dtype=torch.bfloat16) * 0.2)(B, S, D)),
            "shift_k": _w((lambda *s: torch.randn(*s, dtype=torch.bfloat16) * 0.2)(D, ks) * 0.5),
            "ssm_k": _w((lambda *s: torch.randn(*s, dtype=torch.bfloat16) * 0.8)(D, S) * (1.0 / S ** 0.5)),
            "ssm_D": _w((lambda *s: torch.randn(*s, dtype=torch.bfloat16) * 0.2)(D)),
        }
    _A_in = _A_setup()

    def _B_setup():
        B, S, D, F = 1, 2048, 512, 1792
        return {
            "x": _w((lambda *s: torch.randn(*s, dtype=torch.bfloat16) * 0.05)(B, S, D)),
            "w1": _w((lambda *s: torch.randn(*s, dtype=torch.bfloat16) * 0.8)(D, F) * (D ** -0.5)),
            "b1": _w((lambda *s: torch.randn(*s, dtype=torch.bfloat16) * 0.4)(F)),
            "w2": _w((lambda *s: torch.randn(*s, dtype=torch.bfloat16) * 0.05)(F, D) * (F ** -0.5)),
            "b2": _w((lambda *s: torch.randn(*s, dtype=torch.bfloat16) * 0.05)(D)),
        }
    _B_in = _B_setup()

    return {
        "A_q": _A_in["q"],
        "A_k": _A_in["k"],
        "A_v": _A_in["v"],
        "A_shift_k": _A_in["shift_k"],
        "A_ssm_k": _A_in["ssm_k"],
        "A_ssm_D": _A_in["ssm_D"],
        "B_x": _B_in["x"],
        "B_w1": _B_in["w1"],
        "B_b1": _B_in["b1"],
        "B_w2": _B_in["w2"],
        "B_b2": _B_in["b2"],
    }

def reference(A_q, A_k, A_v, A_shift_k, A_ssm_k, A_ssm_D, B_x, B_w1, B_b1, B_w2, B_b2):
    def _A_ref(q, k, v, shift_k, ssm_k, ssm_D):
        B, S, D = q.shape
        ks = shift_k.shape[-1]
        k_pad = _pad(k, ((0, 0), (ks - 1, 0), (0, 0)))
        k_shift = torch.zeros_like(k)
        for t in range(ks):
            k_shift = k_shift + k_pad[:, t:t + S, :] * shift_k[:, ks - 1 - t][None, None, :]
        kv = k_shift * v
        L = 2 * S
        kv_pad = _pad(kv, ((0, 0), (0, S), (0, 0)))
        kf_pad = _pad(ssm_k, ((0, 0), (0, S)))
        kv_f = (lambda x, axes=(-2,-1): torch.fft.rfft2(x.float(), dim=axes))(kv_pad[:, None, :, :], axes=(1, 2))
        kf_f = (lambda x, axes=(-2,-1): torch.fft.rfft2(x.float(), dim=axes))(kf_pad[:, None, :], axes=(1, 2))
        kf_f = (lambda x, *d: x.permute(*d))(kf_f, 1, 2, 0)
        y_f = kv_f * kf_f[None, :, :, :]
        y = (lambda x, s, axes=(-2,-1): torch.fft.irfft2(x, s=s, dim=axes).to(torch.bfloat16))(y_f, (1, L), axes=(1, 2))[:, 0, :S, :]
        y = y + kv * ssm_D[None, None, :]
        return q * y

    def _B_ref(x, w1, b1, w2, b2):
        y = _layernorm(x, axis=-1)
        h = _gelu(torch.einsum("bsd,df->bsf", y, w1) + b1)
        return torch.einsum("bsf,fd->bsd", h, w2) + b2

    x = A_q
    h = x + _A_ref(_layernorm(x, axis=-1), A_k, A_v, A_shift_k, A_ssm_k, A_ssm_D)
    h2 = h + _B_ref(_layernorm(h, axis=-1), B_w1, B_b1, B_w2, B_b2)
    return h2
```

```cpp
#define CODE_PREFETCH 1
#define STAMPS 1
#define DIAG_I 0
#define DIAG_J 12
#include <hip/hip_runtime.h>
#include <stdint.h>

typedef unsigned short u16;
typedef unsigned int u32;
typedef unsigned long long u64;
typedef u32 u32x2 __attribute__((ext_vector_type(2)));
typedef u32 u32x4 __attribute__((ext_vector_type(4)));
typedef float f32x2 __attribute__((ext_vector_type(2)));
typedef float f32x4 __attribute__((ext_vector_type(4)));
typedef float f32x16 __attribute__((ext_vector_type(16)));
typedef short s16x4 __attribute__((ext_vector_type(4)));
typedef short bf16x8 __attribute__((ext_vector_type(8)));
typedef __bf16 bf16x2_t __attribute__((ext_vector_type(2)));
#define LAS __attribute__((address_space(3)))
#define GAS __attribute__((address_space(1)))
#define DI __device__ __forceinline__

constexpr int SEQ = 2048, DM = 512, FF = 1792;
constexpr int NWG = 256, NT = 512;
constexpr size_t WS_GEN = 0;
constexpr size_t WS_C1 = 81920;
constexpr size_t WS_C2 = WS_C1 + 16 * 256;
constexpr size_t WS_C3 = WS_C2 + 16 * 256;
constexpr size_t WS_R1 = WS_C3 + 16 * 256;
constexpr size_t WS_R2 = WS_R1 + 32 * 256;
constexpr size_t WS_R3 = WS_R2 + 32 * 256;
constexpr size_t WS_STATS = 16384;
constexpr size_t WS_CS = 65536;
constexpr size_t WS_RS = 32768;
constexpr size_t WS_KVR = 131072;
constexpr size_t WS_XT = WS_KVR + (size_t)DM * SEQ * 2;
constexpr size_t WS_HT = WS_XT + (size_t)DM * SEQ * 2;
constexpr size_t WS_H = WS_HT + (size_t)DM * SEQ * 2;
constexpr size_t WS_STAMP = WS_H + (size_t)SEQ * FF * 2;
constexpr u32 LDS_BYTES = 163840;
constexpr u32 L_RB = 65536;
constexpr u32 L_F = L_RB;
constexpr u32 F_CH = 1056 * 16;
constexpr u32 L_X = L_F + 2 * F_CH;
constexpr u32 X_ST = 4576;
constexpr u32 L_P = L_X + 4 * X_ST;
constexpr u32 P_ST = 9216;
constexpr u32 L_KV = L_P, L_XI = L_P + 8192;
static_assert(L_P % 16 == 0 && L_P + 4 * P_ST <= LDS_BYTES, "lds");
constexpr u32 L_TAB = 9 * 16384;
constexpr u32 L_HT2 = 131072;

struct Params {
    const u16 *x, *k, *v, *sk, *ssm, *sD, *w1, *b1, *w2, *b2;
    u16* out; char* ws;
};

DI float bflo(u32 d) { return __uint_as_float(d << 16); }
DI float bfhi(u32 d) { return __uint_as_float(d & 0xffff0000u); }
DI u32 pkbf(float lo, float hi) { typedef __bf16 b2 __attribute__((ext_vector_type(2))); f32x2 v = {lo, hi}; b2 r = __builtin_convertvector(v, b2); return __builtin_bit_cast(u32, r); }

DI u32x4 gld4(const void* sb, u32 vo) { u32x4 r; asm volatile("global_load_dwordx4 %0, %1, %2" : "=v"(r) : "v"(vo), "s"(sb) : "memory"); return r; }
DI u32x4 gld4c(const void* sb, u32 vo) { u32x4 r; asm volatile("global_load_dwordx4 %0, %1, %2 sc1" : "=v"(r) : "v"(vo), "s"(sb) : "memory"); return r; }
DI u32x2 gld2(const void* sb, u32 vo) { u32x2 r; asm volatile("global_load_dwordx2 %0, %1, %2" : "=v"(r) : "v"(vo), "s"(sb) : "memory"); return r; }
DI u32 gld1c(const void* sb, u32 vo) { u32 r; asm volatile("global_load_dword %0, %1, %2 sc1" : "=v"(r) : "v"(vo), "s"(sb) : "memory"); return r; }
DI void gst4c(void* sb, u32 vo, u32x4 d) { asm volatile("global_store_dwordx4 %0, %1, %2 sc1\n\ts_nop 1" :: "v"(vo), "v"(d), "s"(sb) : "memory"); }
DI void gst2c(void* sb, u32 vo, u32x2 d) { asm volatile("global_store_dwordx2 %0, %1, %2 sc1\n\ts_nop 1" :: "v"(vo), "v"(d), "s"(sb) : "memory"); }
DI void gst2(void* sb, u32 vo, u32x2 d) { asm volatile("global_store_dwordx2 %0, %1, %2\n\ts_nop 1" :: "v"(vo), "v"(d), "s"(sb) : "memory"); }
DI void dma(const void* sb, u32 vo, u32 lds) {
    u32 keep;
    asm volatile("s_mov_b32 %0, m0\n\ts_mov_b32 m0, %3\n\ts_nop 0\n\tglobal_load_lds_dwordx4 %1, %2\n\ts_mov_b32 m0, %0" : "=&s"(keep) : "v"(vo), "s"(sb), "s"(lds) : "memory");
}
DI void dmac(const void* sb, u32 vo, u32 lds) {
    u32 keep;
    asm volatile("s_mov_b32 %0, m0\n\ts_mov_b32 m0, %3\n\ts_nop 0\n\tglobal_load_lds_dwordx4 %1, %2 sc1\n\ts_mov_b32 m0, %0" : "=&s"(keep) : "v"(vo), "s"(sb), "s"(lds) : "memory");
}
template <int N> DI void wait_vm() { asm volatile("s_waitcnt vmcnt(%0)" :: "n"(N) : "memory"); }
#define PIN(x) asm volatile("" : "+v"(x))
DI void bar() { asm volatile("s_waitcnt lgkmcnt(0)\n\ts_barrier" ::: "memory"); }

template <int CTRL, int RM> DI float dpp_add(float v) {
    const int t = __builtin_amdgcn_update_dpp(0, __float_as_int(v), CTRL, RM, 0xf, true);
    return v + __int_as_float(t);
}
DI float wave_sum(float v) {
    v = dpp_add<0x111, 0xf>(v); v = dpp_add<0x112, 0xf>(v); v = dpp_add<0x114, 0xf>(v); v = dpp_add<0x118, 0xf>(v);
    v = dpp_add<0x142, 0xa>(v); v = dpp_add<0x143, 0xc>(v);
    return __int_as_float(__builtin_amdgcn_readlane(__float_as_int(v), 63));
}
DI s16x4 tr_read(const LAS char* p) { return __builtin_amdgcn_ds_read_tr16_b64_v4i16((LAS s16x4*)p); }
DI bf16x8 cat8(s16x4 a, s16x4 b) { return __builtin_shufflevector(a, b, 0, 1, 2, 3, 4, 5, 6, 7); }
DI float dot2(u32 a, u32 b, float c) { return __builtin_amdgcn_fdot2_f32_bf16(__builtin_bit_cast(bf16x2_t, a), __builtin_bit_cast(bf16x2_t, b), c, false); }

#define STAMP(i) do { if (threadIdx.x == 0) { ((LAS u64*)(L + 160000))[i] = __builtin_amdgcn_s_memrealtime(); if ((i) == 0 || (i) == 15) ((LAS u64*)(L + 160000))[(i) == 0 ? 30 : 31] = __builtin_amdgcn_s_memtime(); } } while (0)
#define STAMPV(i, v) do { if (threadIdx.x == 0) ((LAS u64*)(L + 160000))[i] = (v); } while (0)

constexpr u64 SPIN_TICKS = 400000;
DI int poll_tag(const char* base, u32 vo, u64 tag) {
    const u64 t0 = __builtin_amdgcn_s_memrealtime(); int iters = 0;
    for (;;) {
        u32x2 a;
        asm volatile("global_load_dwordx2 %0, %1, %2 sc1\n\ts_waitcnt vmcnt(0)" : "=&v"(a) : "v"(vo), "s"(base) : "memory");
        ++iters;
        if (__all(a.x == (u32)tag && a.y == (u32)(tag >> 32))) break;
        if (__builtin_amdgcn_s_memrealtime() - t0 > SPIN_TICKS) break;
    }
    return iters;
}
DI void arrive_all(char* shard_cnt, char* top_cnt, char* rel, u64 tag, int lane) {
    u32 old = 0;
    if (lane == 0) old = __hip_atomic_fetch_add((GAS u32*)shard_cnt, 1u, __ATOMIC_RELAXED, __HIP_MEMORY_SCOPE_AGENT);
    if (__builtin_amdgcn_readfirstlane(old) == 31u) {
        u32 o2 = 0;
        if (lane == 0) o2 = __hip_atomic_fetch_add((GAS u32*)top_cnt, 1u, __ATOMIC_RELAXED, __HIP_MEMORY_SCOPE_AGENT);
        if (__builtin_amdgcn_readfirstlane(o2) == 7u) { if (lane < 32) __hip_atomic_store((GAS u64*)(rel + lane * 256), tag, __ATOMIC_RELAXED, __HIP_MEMORY_SCOPE_AGENT); }
    }
}
DI void arrive_tile(char* cnt, u32 last, char* rel, u64 tag, int lane) {
    u32 old = 0;
    if (lane == 0) old = __hip_atomic_fetch_add((GAS u32*)cnt, 1u, __ATOMIC_RELAXED, __HIP_MEMORY_SCOPE_AGENT);
    if (__builtin_amdgcn_readfirstlane(old) == last) { if (lane == 0) __hip_atomic_store((GAS u64*)rel, tag, __ATOMIC_RELAXED, __HIP_MEMORY_SCOPE_AGENT); }
}

DI u32 swz(u32 row) { return ((row & 3) << 2) | ((row >> 2) & 3); }

__global__ void __launch_bounds__(NT, 2) fused_kernel(Params p) {
    extern __shared__ __attribute__((aligned(16))) char smem_[];
    LAS char* const L = (LAS char*)smem_;
    const int tid = threadIdx.x, lane = tid & 63;
    const int w = __builtin_amdgcn_readfirstlane(tid >> 6);
    const int b = blockIdx.x, xg = b & 7, jb = b >> 3;
    STAMP(0);
    u64 tag; asm volatile("s_load_dwordx2 %0, %1, 0x0" : "=s"(tag) : "s"(p.ws + WS_GEN) : "memory");
    u32 cpf = 0;
    {
        u64 pc; asm volatile("s_getpc_b64 %0" : "=s"(pc));
        asm volatile("global_load_dword %0, %1, %2" : "=v"(cpf) : "v"((tid & 255) * 128), "s"(pc) : "memory");
    }

    const bool g1 = jb < 28;
    const int rt = 2 * xg + (jb >= 14 ? 1 : 0), ct = jb >= 28 ? 13 : jb >= 14 ? jb - 14 : jb;
    const u32 prow = 4 * w + (lane >> 4);
    const u32 pch = ((lane & 15) ^ swz(prow)) * 16;
    auto stage_w1 = [&](int kc, int slot) {
        const char* g = (const char*)p.w1 + (size_t)kc * 64 * FF * 2 + ct * 256;
        dma(g, prow * (FF * 2) + pch, slot * 16384 + w * 1024);
        dma(g, (prow + 32) * (FF * 2) + pch, slot * 16384 + 8192 + w * 1024);
    };
    auto stage_h = [&](int kc, int slot) {
        const char* g = p.ws + WS_HT + (size_t)rt * 131072 + kc * 16384;
        dmac(g, prow * 256 + pch, slot * 16384 + w * 1024);
        dmac(g, (prow + 32) * 256 + pch, slot * 16384 + 8192 + w * 1024);
    };

    u32x2 kr[5], vr[2]; u32x4 skv[2], xrow, fhead;
    {
        const int rp = tid >> 4, c4 = tid & 15, s = 64 * jb + 2 * rp;
        const u32 co = (64 * xg + 4 * c4) * 2;
#pragma unroll
        for (int j = 0; j < 5; ++j) { const int r = s - 3 + j; kr[j] = gld2(p.k, (r < 0 ? 0 : r) * 1024 + co); }
        vr[0] = gld2(p.v, s * 1024 + co); vr[1] = gld2(p.v, s * 1024 + 1024 + co);
        skv[0] = gld4(p.sk, co * 4); skv[1] = gld4(p.sk, co * 4 + 16);
        dma(p.x, (64 * jb + 8 * w + (lane >> 3)) * 1024 + xg * 128 + (lane & 7) * 16, L_XI + w * 1024);
        xrow = gld4(p.x, (8 * b + w) * 1024 + lane * 16);
    }
    u32 tw0 = 0, tw1 = 0;
    const int ca = 64 * xg + 2 * jb;
    {
        const int cc = w >> 2, cp = w & 3;
        const u32 rowb = (u32)(ca + cc) * 4096;
        const u32 dst = L_F + cc * F_CH + (260 * cp + (cp > 0 ? 1 : 0)) * 16;
#pragma unroll
        for (int pc = 0; pc < 4; ++pc) {
            u32 o = 16 * (64 * pc + lane) + 4 * cp; if (o > 4080) o = 4080;
            dma(p.ssm, rowb + o, dst + 64 + pc * 1024);
        }
        fhead = gld4(p.ssm, rowb);
#define PT 0
        if (tid < 112) { const int cpy = tid / 28, ch = tid - cpy * 28; *(LAS u32x4*)(L + L_X + cpy * X_ST + (256 + ch) * 16) = (u32x4){0, 0, 0, 0}; }
    }
    asm volatile("s_waitcnt lgkmcnt(0)" : "+s"(tag) :: "memory");
    tag += 1;
    if (b < 8 && w == 7) {
        if (lane < (b == 0 ? 2 : 1)) gst4c(p.ws + WS_C1, lane == 0 ? b * 256 : 8 * 256, (u32x4){(u32)tag, (u32)(tag >> 32), 0u, 0u});
        if (lane < (b == 0 ? 4 : 3)) gst2c(p.ws + WS_C2, lane == 0 ? b * 256 : lane == 3 ? 8 * 256 : 4096 + (2 * b + lane - 1) * 256, (u32x2){0u, 0u});
    }
    {
        const int rp = tid >> 4, c4 = tid & 15, s = 64 * jb + 2 * rp;
        asm volatile("s_waitcnt vmcnt(%10)" : "+v"(kr[0]), "+v"(kr[1]), "+v"(kr[2]), "+v"(kr[3]), "+v"(kr[4]), "+v"(vr[0]), "+v"(vr[1]), "+v"(skv[0]), "+v"(skv[1]), "+v"(cpf) : "n"(7 + PT) : "memory");
        STAMP(1);
#pragma unroll
        for (int j = 0; j < 3; ++j) if (s - 3 + j < 0) kr[j] = (u32x2){0, 0};
        f32x2 kf[5][2], wf[4][2];
#pragma unroll
        for (int j = 0; j < 5; ++j) { kf[j][0] = (f32x2){bflo(kr[j].x), bfhi(kr[j].x)}; kf[j][1] = (f32x2){bflo(kr[j].y), bfhi(kr[j].y)}; }
#pragma unroll
        for (int q = 0; q < 2; ++q) {
            wf[0][q] = (f32x2){bflo(skv[q].x), bflo(skv[q].z)}; wf[1][q] = (f32x2){bfhi(skv[q].x), bfhi(skv[q].z)};
            wf[2][q] = (f32x2){bflo(skv[q].y), bflo(skv[q].w)}; wf[3][q] = (f32x2){bfhi(skv[q].y), bfhi(skv[q].w)};
        }
#pragma unroll
        for (int o = 0; o < 2; ++o) {
            u32x2 r;
#pragma unroll
            for (int q = 0; q < 2; ++q) {
                f32x2 a = kf[3 + o][q] * wf[0][q];
                a = kf[2 + o][q] * wf[1][q] + a; a = kf[1 + o][q] * wf[2][q] + a; a = kf[o][q] * wf[3][q] + a;
                const u32 vd = q ? vr[o].y : vr[o].x;
                a = a * (f32x2){bflo(vd), bfhi(vd)};
                if (q) r.y = pkbf(a.x, a.y); else r.x = pkbf(a.x, a.y);
            }
            *(LAS u32x2*)(L + L_KV + (2 * rp + o) * 128 + c4 * 8) = r;
        }
        asm volatile("s_waitcnt vmcnt(%1)" : "+v"(xrow) : "n"(5 + PT) : "memory");
        bar();
        {
            const int cb = w & 3, rc = 4 * (w >> 2) + (lane >> 4), i = lane & 15, q = i >> 2, pp = lane & 3;
            const u32 cofs = (16 * cb + 4 * pp) * 2;
            const s16x4 a0 = tr_read(L + L_KV + (8 * rc + 7 - q) * 128 + cofs), a1 = tr_read(L + L_KV + (8 * rc + 3 - q) * 128 + cofs);
            const s16x4 x0 = tr_read(L + L_XI + (8 * rc + q) * 128 + cofs), x1 = tr_read(L + L_XI + (8 * rc + 4 + q) * 128 + cofs);
            const u32 c = 64 * xg + 16 * cb + i;
            gst4c(p.ws + WS_KVR, c * 4096 + (2040 - 64 * jb - 8 * rc) * 2, __builtin_bit_cast(u32x4, cat8(a0, a1)));
            gst4c(p.ws + WS_XT, c * 4096 + (64 * jb + 8 * rc) * 2, __builtin_bit_cast(u32x4, cat8(x0, x1)));
        }
        float s1 = 0.f, s2 = 0.f;
#pragma unroll
        for (int i = 0; i < 4; ++i) { const float a = bflo(xrow[i]), c = bfhi(xrow[i]); s1 += a + c; s2 += a * a + c * c; }
        s1 = wave_sum(s1); s2 = wave_sum(s2);
        const float mean = s1 * (1.0f / DM);
        const float var = fmaxf(s2 * (1.0f / DM) - mean * mean, 0.f);
        const float rstd = __builtin_amdgcn_rsqf(var + 1e-5f);
        if (lane == 0) gst2c(p.ws + WS_STATS, (w >> 1) * 4096 + (255 - b) * 16 + (w & 1) * 8, (u32x2){__float_as_uint(mean), __float_as_uint(rstd)});
    }
    {
        asm volatile("s_waitcnt vmcnt(%1)" : "+v"(fhead) : "n"(3 + PT) : "memory");
        const int cc = w >> 2, cp = w & 3;
        if (lane < 4) {
            u32x4 h = {0, 0, 0, 0};
            if (lane == 3) {
                h.w = cp >= 1 ? (cp == 1 ? fhead.x : cp == 2 ? fhead.y : fhead.z) : 0u;
                h.z = cp >= 2 ? (cp == 2 ? fhead.x : fhead.y) : 0u;
                h.y = cp >= 3 ? fhead.x : 0u;
            }
            *(LAS u32x4*)(L + L_F + cc * F_CH + (260 * cp + (cp > 0 ? 1 : 0)) * 16 + lane * 16) = h;
        }
    }
    if (w == 0) poll_tag(p.ws + WS_C1, (lane & 1) ? 8 * 256 : xg * 256, tag);
    asm volatile("s_waitcnt vmcnt(0)" : "+v"(tw0), "+v"(tw1) :: "memory");
    STAMP(2);
    bar();
    if (w == 0) arrive_all(p.ws + WS_C1 + xg * 256 + 8, p.ws + WS_C1 + 8 * 256 + 8, p.ws + WS_R1, tag, lane);
    STAMP(3);

    const int h5 = lane >> 5, blk = (lane >> 4) & 1, tq = (lane & 15) >> 2, tp = lane & 3;
    const int fq = w & 3, rh = w >> 2;
    u32 ao[2];
#pragma unroll
    for (int t = 0; t < 2; ++t) { const u32 row = 8 * h5 + 4 * t + tq; ao[t] = 256 * row + 16 * ((4 * fq + 2 * blk + (tp >> 1)) ^ swz(row)) + 8 * (tp & 1); }
    bf16x8 wa[32];
    {
        if (w == 0) { const int it = poll_tag(p.ws + WS_R1, jb * 256, tag); STAMPV(27, it); }
        bar();
        STAMP(4);
        const int cc = tid >> 8, j8 = tid & 255;
        const u32 crow = (u32)(ca + cc) * 4096;
        const int s0 = 2040 - 8 * j8;
        u32x4 kvc = gld4c(p.ws + WS_KVR, crow + j8 * 16);
        u32 kvn = gld1c(p.ws + WS_KVR, crow + (j8 < 255 ? j8 * 16 + 16 : 0));
        u32x4 xc = gld4c(p.ws + WS_XT, crow + s0 * 2);
        u32x4 st[4];
#pragma unroll
        for (int i = 0; i < 4; ++i) st[i] = gld4c(p.ws + WS_STATS, i * 4096 + j8 * 16);
        u32 sdv; asm volatile("global_load_ushort %0, %1, %2" : "=v"(sdv) : "v"((ca + cc) * 2), "s"(p.sD) : "memory");
        asm volatile("s_waitcnt vmcnt(6)" : "+v"(kvc), "+v"(kvn) :: "memory");
        if (j8 == 255) kvn = 0;
        {
            u32x4 sh;
            sh.x = __builtin_amdgcn_alignbit(kvc.y, kvc.x, 16); sh.y = __builtin_amdgcn_alignbit(kvc.z, kvc.y, 16);
            sh.z = __builtin_amdgcn_alignbit(kvc.w, kvc.z, 16); sh.w = __builtin_amdgcn_alignbit(kvn, kvc.w, 16);
            *(LAS u32x4*)(L + L_X + (2 * cc + 1) * X_ST + j8 * 16) = kvc;
            *(LAS u32x4*)(L + L_X + (2 * cc) * X_ST + j8 * 16) = sh;
        }
        bar();
        STAMP(5);
        {
            const int wc = w >> 2, kh = (w >> 1) & 1, ts = w & 1;
            const int m = lane & 15, g = lane >> 4, cp = (m + 1) & 3;
            const LAS char* ab = L + L_F + wc * F_CH + (260 * cp + (cp > 0 ? 1 : 0)) * 16 + 16 * g + 16 * ((m + 1) >> 2) + 256 * kh;
            const LAS char* xb = L + L_X + (2 * wc + (m & 1)) * X_ST + 448 - 64 * (m >> 1) + 16 * g + 256 * kh;
            f32x4 acc[4];
#pragma unroll
            for (int i = 0; i < 4; ++i) acc[i] = (f32x4){0.f, 0.f, 0.f, 0.f};
            if (ts == 0) {
                bf16x8 A[2][8], G[2][8];
#pragma unroll
                for (int i = 0; i < 8; ++i) { A[0][i] = *(const LAS bf16x8*)(ab + 512 * i); G[0][7 - i] = *(const LAS bf16x8*)(xb + 512 * i); }
#pragma unroll
                for (int tt = 0; tt < 4; ++tt) {
                    const int c = tt & 1, n = c ^ 1;
#pragma unroll
                    for (int i = 0; i < 8; ++i) {
                        if (i == 1) stage_w1(tt, tt);
                        if (tt < 3) { A[n][i] = *(const LAS bf16x8*)(ab + 512 * i + 64 * (tt + 1)); G[n][7 - i] = *(const LAS bf16x8*)(xb + 512 * i + 64 * (tt + 1)); }
                        acc[0] = __builtin_amdgcn_mfma_f32_16x16x32_bf16(A[c][i], G[c][7 - i], acc[0], 0, 0, 0);
                        if (i < 7) acc[1] = __builtin_amdgcn_mfma_f32_16x16x32_bf16(A[c][i], G[c][6 - i], acc[1], 0, 0, 0);
                        if (i < 2) acc[2] = __builtin_amdgcn_mfma_f32_16x16x32_bf16(A[c][i], G[c][1 - i], acc[2], 0, 0, 0);
                        if (i < 1) acc[3] = __builtin_amdgcn_mfma_f32_16x16x32_bf16(A[c][i], G[c][0], acc[3], 0, 0, 0);
                        __builtin_amdgcn_sched_barrier(0);
                    }
                }
            } else {
                bf16x8 A[2][6], G[2][6];
#pragma unroll
                for (int i = 0; i < 6; ++i) { A[0][i] = *(const LAS bf16x8*)(ab + 512 * i); G[0][5 - i] = *(const LAS bf16x8*)(xb + 512 * (i + 2)); }
#pragma unroll
                for (int tt = 0; tt < 4; ++tt) {
                    const int c = tt & 1, n = c ^ 1;
#pragma unroll
                    for (int i = 0; i < 6; ++i) {
                        if (i == 1) stage_w1(tt, tt);
                        if (tt < 3) { A[n][i] = *(const LAS bf16x8*)(ab + 512 * i + 64 * (tt + 1)); G[n][5 - i] = *(const LAS bf16x8*)(xb + 512 * (i + 2) + 64 * (tt + 1)); }
                        acc[0] = __builtin_amdgcn_mfma_f32_16x16x32_bf16(A[c][i], G[c][5 - i], acc[0], 0, 0, 0);
                        if (i < 5) acc[1] = __builtin_amdgcn_mfma_f32_16x16x32_bf16(A[c][i], G[c][4 - i], acc[1], 0, 0, 0);
                        if (i < 4) acc[2] = __builtin_amdgcn_mfma_f32_16x16x32_bf16(A[c][i], G[c][3 - i], acc[2], 0, 0, 0);
                        if (i < 3) acc[3] = __builtin_amdgcn_mfma_f32_16x16x32_bf16(A[c][i], G[c][2 - i], acc[3], 0, 0, 0);
                        __builtin_amdgcn_sched_barrier(0);
                    }
                }
            }
            LAS float* pb = (LAS float*)(L + L_P + (2 * kh + wc) * P_ST) + 36 * (m >> 1) + 8 * g + (m & 1);
#pragma unroll
            for (int i = 0; i < 4; ++i) {
                const int q = ts == 0 ? (i == 0 ? 7 : i == 1 ? 6 : i == 2 ? 1 : 0) : 5 - i;
#pragma unroll
                for (int r = 0; r < 4; ++r) pb[36 * 8 * q + 2 * r] = acc[i][r];
            }
        }
        asm volatile("s_waitcnt vmcnt(8)" : "+v"(xc), "+v"(st[0]), "+v"(st[1]), "+v"(st[2]), "+v"(st[3]), "+v"(sdv) :: "memory");
        bar();
        STAMP(6);
        {
            const LAS char* p0 = L + L_P + cc * P_ST + (36 * (s0 >> 5) + (s0 & 31)) * 4;
            const f32x4 ya = *(const LAS f32x4*)p0, yb = *(const LAS f32x4*)(p0 + 16);
            const f32x4 yc = *(const LAS f32x4*)(p0 + 2 * P_ST), yd = *(const LAS f32x4*)(p0 + 2 * P_ST + 16);
            const float dd = bflo(sdv);
            float h[8];
#pragma unroll
            for (int e = 0; e < 8; ++e) {
                const float y = (e < 4 ? ya[e & 3] + yc[e & 3] : yb[e & 3] + yd[e & 3]);
                const u32 kd = kvc[(7 - e) >> 1], xd = xc[e >> 1];
                const float kv = (e & 1) ? bflo(kd) : bfhi(kd);
                const float x = (e & 1) ? bfhi(xd) : bflo(xd);
                const float mean = __uint_as_float(st[e >> 1][(e & 1) * 2]), rstd = __uint_as_float(st[e >> 1][(e & 1) * 2 + 1]);
                const float y2 = y + kv * dd;
                h[e] = x + (x - mean) * rstd * y2;
            }
            gst4c(p.ws + WS_HT, (u32)(s0 >> 7) * 131072 + (u32)(ca + cc) * 256 + (s0 & 127) * 2, (u32x4){pkbf(h[0], h[1]), pkbf(h[2], h[3]), pkbf(h[4], h[5]), pkbf(h[6], h[7])});
        }
        wait_vm<0>();
        bar();
        if (w == 0) arrive_all(p.ws + WS_C2 + xg * 256, p.ws + WS_C2 + 8 * 256, p.ws + WS_R2, tag, lane);
        STAMP(7);
        if (g1) {
#pragma unroll
            for (int s = 0; s < 16; ++s) wa[s] = cat8(tr_read(L + s * 4096 + ao[0]), tr_read(L + s * 4096 + ao[1]));
            bar();
#pragma unroll
            for (int kc = 4; kc < 8; ++kc) stage_w1(kc, kc - 4);
        } else {
            const int ii = 4 * xg + jb - 28, r8 = tid & 7, cg8 = tid >> 3;
            const char* gw = (const char*)p.w1 + (ii < 28 ? ii : 27) * 128;
            u32x4 wv[8];
#pragma unroll
            for (int i = 0; i < 8; ++i) wv[i] = gld4(gw, (8 * cg8 + i) * (FF * 2) + r8 * 16);
            LAS float* RD = (LAS float*)(L + 0);
            asm volatile("s_waitcnt vmcnt(0)" : "+v"(wv[0]), "+v"(wv[1]), "+v"(wv[2]), "+v"(wv[3]), "+v"(wv[4]), "+v"(wv[5]), "+v"(wv[6]), "+v"(wv[7]) :: "memory");
            f32x2 s1[4];
#pragma unroll
            for (int d = 0; d < 4; ++d) s1[d] = (f32x2){0.f, 0.f};
#pragma unroll
            for (int i = 0; i < 8; ++i)
#pragma unroll
                for (int d = 0; d < 4; ++d) s1[d] += (f32x2){bflo(wv[i][d]), bfhi(wv[i][d])};
#pragma unroll
            for (int d = 0; d < 4; ++d)
#pragma unroll
                for (int e = 0; e < 2; ++e) {
                    float a = s1[d][e];
                    a += __shfl_xor(a, 8); a += __shfl_xor(a, 16); a += __shfl_xor(a, 32);
                    if (lane < 8) RD[w * 64 + 8 * r8 + 2 * d + e] = a;
                }
            bar();
            if (tid < 32 && ii < 28) {
                f32x2 t = {0.f, 0.f};
#pragma unroll
                for (int i = 0; i < 8; ++i) t += *(const LAS f32x2*)(RD + i * 64 + 2 * tid);
                gst4c(p.ws + WS_CS, (32 * ii + tid) * 16, (u32x4){__float_as_uint(t.x), __float_as_uint(t.y), (u32)tag, (u32)(tag >> 32)});
            }
        }
    }

    const int rt2 = 4 * xg + (jb >> 3), dt = jb & 7, kq = w >> 1, dh = w & 1;
    const char* w2b = (const char*)p.w2 + (size_t)(448 * kq) * 1024 + (64 * dt + 32 * dh) * 2;
    const u32 w2o = (lane >> 2) * 1024 + (lane & 3) * 16;
    const u32 w2l = w * 2048;
    auto w2pos = [&](int i) { const int j = (i & 15) >> 1; return (u32)((j == 0 ? 3 : j < 6 ? j + 3 : j - 6) * 16384 + (i & 1) * 1024); };
    auto stage_w2 = [&](int i) { dma(w2b + (size_t)i * 16384, w2o, w2pos(i) + w2l); };
    const u32 tro = w2l + (4 * h5 + tq) * 64 + blk * 32 + tp * 8;
    bf16x8 wf[28];
    auto take_w2 = [&](int i) { wf[i] = cat8(tr_read(L + w2pos(i) + tro), tr_read(L + w2pos(i) + tro + 512)); };
    {
        LAS f32x2* TB = (LAS f32x2*)(L + L_TAB);
        u32 b1v; asm volatile("global_load_dword %0, %1, %2" : "=v"(b1v) : "v"((ct * 128 + 2 * (tid & 63)) * 2), "s"(p.b1) : "memory");
        if (w == 0) { const int it = poll_tag(p.ws + WS_R2, jb * 256, tag); STAMPV(28, it); }
        asm volatile("s_waitcnt vmcnt(0)" : "+v"(b1v) :: "memory");
        bar();
        STAMP(8);
        if (g1) {
            const char* hg = p.ws + WS_HT + (size_t)rt * 131072;
            auto stage_hp = [&](int j) { const int kc = j >> 1; dmac(hg + kc * 16384 + (j & 1) * 8192, prow * 256 + pch, (kc < 5 ? kc + 4 : kc - 5) * 16384 + (j & 1) * 8192 + w * 1024); };
#pragma unroll
            for (int j = 0; j < 6; ++j) stage_hp(j);
#pragma unroll
            for (int s = 16; s < 32; ++s) wa[s] = cat8(tr_read(L + (s - 16) * 4096 + ao[0]), tr_read(L + (s - 16) * 4096 + ao[1]));
            u32 bo[2][2];
#pragma unroll
            for (int t = 0; t < 2; ++t) {
                const u32 row = 8 * h5 + 4 * t + tq;
#pragma unroll
                for (int a = 0; a < 2; ++a) bo[a][t] = 256 * row + 16 * ((4 * (2 * rh + a) + 2 * blk + (tp >> 1)) ^ swz(row)) + 8 * (tp & 1);
            }
            f32x16 acc[2];
#pragma unroll
            for (int i = 0; i < 16; ++i) { acc[0][i] = 0.f; acc[1][i] = 0.f; }
            bf16x8 fb[3][2];
            u32x4 rsg[2], csg;
            const LAS char* hb[3][4];
#pragma unroll
            for (int r = 0; r < 3; ++r)
#pragma unroll
                for (int i = 0; i < 4; ++i) { u32 o = bo[i >> 1][i & 1] + r * 65536; asm volatile("" : "+v"(o)); hb[r][i] = L + o; }
            auto ldf = [&](int s) {
                const int kc = s >> 2, ks = s & 3, slot = kc < 5 ? kc + 4 : kc - 5, r = slot >> 2, o = (slot & 3) * 16384 + ks * 4096;
                fb[s % 3][0] = cat8(tr_read(hb[r][0] + o), tr_read(hb[r][1] + o));
                fb[s % 3][1] = cat8(tr_read(hb[r][2] + o), tr_read(hb[r][3] + o));
            };
            wait_vm<4>(); bar();
            STAMP(9);
            ldf(0); ldf(1);
#pragma unroll
            for (int s = 0; s < 32; ++s) {
                if ((s & 3) == 2 && s < 30) {
                    const int kc = (s + 2) >> 2;
                    if (kc == 1) wait_vm<4>(); else if (kc == 2) wait_vm<6>(); else if (kc == 3) wait_vm<8>(); else if (kc == 4) wait_vm<10>(); else if (kc == 5) wait_vm<12>(); else if (kc == 6) wait_vm<14>(); else wait_vm<17>();
                    bar();
                }
                if (s < 10) stage_hp(6 + s);
                else if (s < 22) stage_w2(s - 10);
                else if (s == 22) {
                    rsg[0] = gld4c(p.ws + WS_RS, (128 * rt + 64 * rh + (lane & 31)) * 16); rsg[1] = gld4c(p.ws + WS_RS, (128 * rt + 64 * rh + 32 + (lane & 31)) * 16);
                    csg = gld4c(p.ws + WS_CS, (64 * ct + (tid & 63)) * 16);
                }
                else if (s == 24 || s == 25) stage_w2(s - 12);
                else if (s == 28 || s == 29) stage_w2(s - 14);
                if (s < 30) ldf(s + 2);
                __builtin_amdgcn_sched_barrier(0);
                acc[0] = __builtin_amdgcn_mfma_f32_32x32x16_bf16(wa[s], fb[s % 3][0], acc[0], 0, 0, 0);
                acc[1] = __builtin_amdgcn_mfma_f32_32x32x16_bf16(wa[s], fb[s % 3][1], acc[1], 0, 0, 0);
                __builtin_amdgcn_sched_barrier(0);
            }
            STAMP(10);
            asm volatile("s_waitcnt vmcnt(4)" : "+v"(rsg[0]), "+v"(rsg[1]), "+v"(csg) :: "memory");
            {
                const u32 tl = (u32)tag, th = (u32)(tag >> 32);
                if (!__all(rsg[0].z == tl && rsg[0].w == th && rsg[1].z == tl && rsg[1].w == th && csg.z == tl && csg.w == th)) {
                    const u64 t0 = __builtin_amdgcn_s_memrealtime();
                    do {
                        asm volatile("global_load_dwordx4 %0, %3, %6 sc1\n\tglobal_load_dwordx4 %1, %4, %6 sc1\n\tglobal_load_dwordx4 %2, %5, %7 sc1\n\ts_waitcnt vmcnt(0)"
                                     : "=&v"(rsg[0]), "=&v"(rsg[1]), "=&v"(csg)
                                     : "v"((128 * rt + 64 * rh + (lane & 31)) * 16), "v"((128 * rt + 64 * rh + 32 + (lane & 31)) * 16), "v"((64 * ct + (tid & 63)) * 16), "s"(p.ws + WS_RS), "s"(p.ws + WS_CS) : "memory");
                    } while (!__all(rsg[0].z == tl && rsg[0].w == th && rsg[1].z == tl && rsg[1].w == th && csg.z == tl && csg.w == th) && __builtin_amdgcn_s_memrealtime() - t0 < SPIN_TICKS);
                }
            }
            if (tid < 64) *(LAS f32x4*)(TB + 2 * tid) = (f32x4){__uint_as_float(csg.x), bflo(b1v), __uint_as_float(csg.y), bfhi(b1v)};
            bar();
            STAMP(24);
#pragma unroll
            for (int a = 0; a < 2; ++a) {
                const float rr = __uint_as_float(rsg[a].x), rm = __uint_as_float(rsg[a].y);
                u32 pk[8];
#pragma unroll
                for (int g = 0; g < 4; ++g) {
                    const int f = 32 * fq + 8 * g + 4 * h5;
                    const f32x4 t0 = *(const LAS f32x4*)(TB + f), t1 = *(const LAS f32x4*)(TB + f + 2);
#pragma unroll
                    for (int e = 0; e < 2; ++e) {
                        const f32x4 tt = e ? t1 : t0;
                        const f32x2 c2 = {tt[0], tt[2]}, b2 = {tt[1], tt[3]}, a2 = {acc[a][4 * g + 2 * e], acc[a][4 * g + 2 * e + 1]};
                        const f32x2 u = a2 * rr + (c2 * rm + b2);
                        const f32x2 z = u * (u * u * (-0.1029432f) + (-2.3022082f));
                        f32x2 d; d.x = __builtin_amdgcn_exp2f(z.x); d.y = __builtin_amdgcn_exp2f(z.y);
                        d = d + 1.0f;
                        f32x2 r; r.x = __builtin_amdgcn_rcpf(d.x); r.y = __builtin_amdgcn_rcpf(d.y);
                        r = r * u;
                        pk[2 * g + e] = pkbf(r.x, r.y);
                    }
                }
                const u32 ks0 = (128 * ct + 32 * fq) / 16;
                const u32 off = ((u32)(4 * rt + 2 * rh + a) * 112 + ks0) * 1024 + lane * 16;
                gst4c(p.ws + WS_H, off, (u32x4){pk[0], pk[1], pk[2], pk[3]});
                gst4c(p.ws + WS_H, off + 1024, (u32x4){pk[4], pk[5], pk[6], pk[7]});
            }
            STAMP(25);
            wait_vm<4>();
#pragma unroll
            for (int i = 0; i < 16; ++i) take_w2(i);
            wait_vm<0>();
            STAMP(26);
            bar();
            if (w == 0) arrive_tile(p.ws + WS_C3 + rt * 256, 13u, p.ws + WS_R3 + rt * 256, tag, lane);
        } else {
            const int idx = jb - 28, srt = 2 * xg + (idx >> 1), r8 = tid & 7, cg8 = tid >> 3;
            const char* g = p.ws + WS_HT + (size_t)srt * 131072 + (idx & 1) * 128;
            u32x4 hv[8];
#pragma unroll
            for (int i = 0; i < 8; ++i) hv[i] = gld4c(g, (8 * cg8 + i) * 256 + r8 * 16);
            LAS float* RD = (LAS float*)(L + 0);
            asm volatile("s_waitcnt vmcnt(0)" : "+v"(hv[0]), "+v"(hv[1]), "+v"(hv[2]), "+v"(hv[3]), "+v"(hv[4]), "+v"(hv[5]), "+v"(hv[6]), "+v"(hv[7]) :: "memory");
            {
                f32x2 s1[4], s2[4];
#pragma unroll
                for (int d = 0; d < 4; ++d) { s1[d] = (f32x2){0.f, 0.f}; s2[d] = (f32x2){0.f, 0.f}; }
#pragma unroll
                for (int i = 0; i < 8; ++i)
#pragma unroll
                    for (int d = 0; d < 4; ++d) { const f32x2 v = {bflo(hv[i][d]), bfhi(hv[i][d])}; s1[d] += v; s2[d] += v * v; }
#pragma unroll
                for (int d = 0; d < 4; ++d)
#pragma unroll
                    for (int e = 0; e < 2; ++e) {
                        float a = s1[d][e], q = s2[d][e];
                        a += __shfl_xor(a, 8); q += __shfl_xor(q, 8); a += __shfl_xor(a, 16); q += __shfl_xor(q, 16); a += __shfl_xor(a, 32); q += __shfl_xor(q, 32);
                        if (lane < 8) *(LAS f32x2*)(RD + (w * 64 + 8 * r8 + 2 * d + e) * 2) = (f32x2){a, q};
                    }
            }
            bar();
            if (tid < 64) {
                f32x2 t = {0.f, 0.f};
#pragma unroll
                for (int i = 0; i < 8; ++i) t += *(const LAS f32x2*)(RD + (i * 64 + tid) * 2);
                const float mean = t.x * (1.0f / DM);
                const float var = fmaxf(t.y * (1.0f / DM) - mean * mean, 0.f);
                const float r1 = __builtin_amdgcn_rsqf(var + 1e-5f);
                const float r2 = __builtin_amdgcn_rsqf(var * r1 * r1 + 1e-5f);
                const float rr = r1 * r2;
                gst4c(p.ws + WS_RS, (128 * srt + 64 * (idx & 1) + tid) * 16, (u32x4){__float_as_uint(rr), __float_as_uint(-rr * mean), (u32)tag, (u32)(tag >> 32)});
            }
            bar();
#pragma unroll
            for (int i = 0; i < 16; ++i) stage_w2(i);
            wait_vm<0>();
#pragma unroll
            for (int i = 0; i < 16; ++i) take_w2(i);
        }
        STAMP(11);
    }

    {
        asm volatile("s_waitcnt lgkmcnt(0)" ::: "memory");
#pragma unroll
        for (int i = 0; i < 16; ++i) PIN(wf[i]);
#pragma unroll
        for (int i = 16; i < 28; ++i) stage_w2(i);
        u32x2 bv = gld2(p.b2, (64 * dt + 32 * dh + 8 * kq + 4 * h5) * 2);
        if (w == 0) { const int it = poll_tag(p.ws + WS_R3, (rt2 >> 1) * 256, tag); STAMPV(29, it); }
        asm volatile("s_waitcnt vmcnt(0)" : "+v"(bv) :: "memory");
#pragma unroll
        for (int i = 16; i < 28; ++i) take_w2(i);
        bar();
        STAMP(12);
        const char* hb = p.ws + WS_H + ((size_t)(2 * rt2 + dh) * 112 + 28 * kq) * 1024;
        auto stage_b = [&](int j) { dmac(hb + (size_t)j * 1024, lane * 16, ((j >> 2) & 3) * 32768 + (kq * 2 + dh) * 4096 + (j & 3) * 1024); };
#pragma unroll
        for (int j = 0; j < 12; ++j) stage_b(j);
        f32x16 acc[2];
#pragma unroll
        for (int i = 0; i < 16; ++i) { acc[0][i] = 0.f; acc[1][i] = 0.f; }
        bf16x8 fb[3][2];
        const LAS char* sb[2];
#pragma unroll
        for (int r = 0; r < 2; ++r) { u32 o = kq * 8192 + lane * 16 + r * 65536; asm volatile("" : "+v"(o)); sb[r] = L + o; }
        auto ldf = [&](int s) {
            const int slot = (s >> 2) & 3, o = (slot & 1) * 32768 + (s & 3) * 1024;
            fb[s % 3][0] = *(const LAS bf16x8*)(sb[slot >> 1] + o); fb[s % 3][1] = *(const LAS bf16x8*)(sb[slot >> 1] + o + 4096);
        };
        wait_vm<8>(); bar();
        STAMP(13);
        ldf(0); ldf(1);
#pragma unroll
        for (int s = 0; s < 28; ++s) {
            if ((s & 3) == 2 && s < 26) {
                const int bt = (s + 2) >> 2;
                if (bt <= 4) wait_vm<6>(); else if (bt == 5) wait_vm<5>(); else wait_vm<1>();
                bar();
            }
            if (s < 16) stage_b(12 + s);
            if (s == 16) dmac(p.ws + WS_HT, (u32)(rt2 >> 1) * 131072 + (64 * dt + 8 * w + (lane >> 3)) * 256 + (rt2 & 1) * 128 + (lane & 7) * 16, L_HT2 + w * 1024);
            if (s < 26) ldf(s + 2);
            __builtin_amdgcn_sched_barrier(0);
            acc[0] = __builtin_amdgcn_mfma_f32_32x32x16_bf16(wf[s], fb[s % 3][0], acc[0], 0, 0, 0);
            acc[1] = __builtin_amdgcn_mfma_f32_32x32x16_bf16(wf[s], fb[s % 3][1], acc[1], 0, 0, 0);
            __builtin_amdgcn_sched_barrier(0);
        }
        wait_vm<0>();
        STAMP(14);
        bar();
#pragma unroll
        for (int g = 0; g < 4; ++g) {
            if (g != kq) {
#pragma unroll
                for (int a = 0; a < 2; ++a)
                    *(LAS f32x4*)(L + (((g * 4 + kq) * 2 + dh) * 2 + a) * 1024 + lane * 16) = (f32x4){acc[a][4 * g], acc[a][4 * g + 1], acc[a][4 * g + 2], acc[a][4 * g + 3]};
            }
        }
        bar();
        {
            const int d0 = 32 * dh + 8 * kq + 4 * h5;
#pragma unroll
            for (int a = 0; a < 2; ++a) {
                f32x4 s;
                if (kq == 0) s = (f32x4){acc[a][0], acc[a][1], acc[a][2], acc[a][3]};
                else if (kq == 1) s = (f32x4){acc[a][4], acc[a][5], acc[a][6], acc[a][7]};
                else if (kq == 2) s = (f32x4){acc[a][8], acc[a][9], acc[a][10], acc[a][11]};
                else s = (f32x4){acc[a][12], acc[a][13], acc[a][14], acc[a][15]};
#pragma unroll
                for (int o = 1; o < 4; ++o) { const int src = (kq + o) & 3; s += *(const LAS f32x4*)(L + (((kq * 4 + src) * 2 + dh) * 2 + a) * 1024 + lane * 16); }
                const s16x4 hv = tr_read(L + L_HT2 + (32 * dh + 8 * kq + 4 * h5 + tq) * 128 + (32 * a + 16 * blk + 4 * tp) * 2);
                const u32x2 hu = __builtin_bit_cast(u32x2, hv);
                const float o0 = s[0] + bflo(bv.x) + bflo(hu.x), o1 = s[1] + bfhi(bv.x) + bfhi(hu.x), o2 = s[2] + bflo(bv.y) + bflo(hu.y), o3 = s[3] + bfhi(bv.y) + bfhi(hu.y);
                gst2(p.out, (u32)(64 * rt2 + 32 * a + (lane & 31)) * 1024 + (64 * dt + d0) * 2, (u32x2){pkbf(o0, o1), pkbf(o2, o3)});
            }
        }
        STAMP(15);
    }
    if (b == 0 && tid == 0) __hip_atomic_store((GAS u64*)(p.ws + WS_GEN), tag, __ATOMIC_RELAXED, __HIP_MEMORY_SCOPE_AGENT);
    if (b == 0 && tid == 0) {
        asm volatile("s_waitcnt vmcnt(0) lgkmcnt(0)" ::: "memory");
        const float us = (float)(((LAS u64*)(L + 160000))[DIAG_J] - ((LAS u64*)(L + 160000))[DIAG_I]) * 0.01f;
        p.out[0] = (u16)(__float_as_uint(us) >> 16);
    }
}

extern "C" void kernel_launch(void* const* d_in, const int* in_sizes, int n_in, void* d_out, int out_size, void* d_ws, size_t ws_size, hipStream_t stream) {
    Params p;
    p.x = (const u16*)d_in[0]; p.k = (const u16*)d_in[1]; p.v = (const u16*)d_in[2]; p.sk = (const u16*)d_in[3]; p.ssm = (const u16*)d_in[4]; p.sD = (const u16*)d_in[5];
    p.w1 = (const u16*)d_in[7]; p.b1 = (const u16*)d_in[8]; p.w2 = (const u16*)d_in[9]; p.b2 = (const u16*)d_in[10];
    p.out = (u16*)d_out; p.ws = (char*)d_ws;
    (void)hipFuncSetAttribute((const void*)fused_kernel, hipFuncAttributeMaxDynamicSharedMemorySize, LDS_BYTES);
    fused_kernel<<<NWG, NT, LDS_BYTES, stream>>>(p);
}
```

```cpp
#include <hip/hip_runtime.h>
#include <stdint.h>
#include <utility>

typedef unsigned short u16;
typedef unsigned int u32;
typedef unsigned long long u64;
typedef u32 u32x2 __attribute__((ext_vector_type(2)));
typedef u32 u32x4 __attribute__((ext_vector_type(4)));
typedef float f32x2 __attribute__((ext_vector_type(2)));
typedef float f32x4 __attribute__((ext_vector_type(4)));
typedef float f32x16 __attribute__((ext_vector_type(16)));
typedef short s16x4 __attribute__((ext_vector_type(4)));
typedef short bf16x8 __attribute__((ext_vector_type(8)));
typedef __bf16 bf16x2_t __attribute__((ext_vector_type(2)));
#define LAS __attribute__((address_space(3)))
#define GAS __attribute__((address_space(1)))
#define DI __device__ __forceinline__

constexpr int SEQ = 2048, DM = 512, FF = 1792;
constexpr int NWG = 256, NT = 512;
constexpr size_t WS_GEN = 0;
constexpr size_t WS_C1 = 65536;
constexpr size_t WS_C2 = WS_C1 + 40 * 256;
constexpr size_t WS_C3 = WS_C2 + 40 * 256;
constexpr size_t WS_R1 = WS_C3 + 32 * 256;
constexpr size_t WS_R2 = WS_R1 + 32 * 256;
constexpr size_t WS_R3 = WS_R2 + 32 * 256;
constexpr size_t WS_STATS = 16384;
constexpr size_t WS_RS = 32768;
constexpr size_t WS_KVR = 131072;
constexpr size_t WS_XT = WS_KVR + (size_t)DM * SEQ * 2;
constexpr size_t WS_HT = WS_XT + (size_t)DM * SEQ * 2;
constexpr size_t WS_H = WS_HT + (size_t)DM * SEQ * 2;
constexpr size_t WS_W2T = WS_H + (size_t)SEQ * FF * 2;
constexpr u32 LDS_BYTES = 163840;
constexpr u32 L_F = 65536;
constexpr u32 F_CH = 1056 * 16;
constexpr u32 L_X = L_F + 2 * F_CH;
constexpr u32 X_ST = 4576;
constexpr u32 L_P = L_X + 4 * X_ST;
constexpr u32 P_ST = 9216;
constexpr u32 L_KV = L_P, L_XI = L_P + 8192;
static_assert(L_P % 16 == 0 && L_P + 4 * P_ST <= LDS_BYTES, "lds");
constexpr u32 L_TAB = 9 * 16384;
constexpr u32 L_R0 = 114688;
constexpr u32 L_HT2 = 147456;
constexpr u32 L_W2S = 154624;

struct Params {
    const u16 *x, *k, *v, *sk, *ssm, *sD, *w1, *b1, *w2, *b2;
    u16* out; char* ws;
};

template <class F, int... I> DI void static_for_impl(F&& f, std::integer_sequence<int, I...>) { (f(std::integral_constant<int, I>{}), ...); }
template <int N, class F> DI void static_for(F&& f) { static_for_impl(f, std::make_integer_sequence<int, N>{}); }
DI float bflo(u32 d) { return __uint_as_float(d << 16); }
DI float bfhi(u32 d) { return __uint_as_float(d & 0xffff0000u); }
DI u32 pkbf(float lo, float hi) { typedef __bf16 b2 __attribute__((ext_vector_type(2))); f32x2 v = {lo, hi}; b2 r = __builtin_convertvector(v, b2); return __builtin_bit_cast(u32, r); }

DI u32x4 gld4(const void* sb, u32 vo) { u32x4 r; asm volatile("global_load_dwordx4 %0, %1, %2" : "=v"(r) : "v"(vo), "s"(sb) : "memory"); return r; }
DI u32x4 gld4c(const void* sb, u32 vo) { u32x4 r; asm volatile("global_load_dwordx4 %0, %1, %2 sc1" : "=v"(r) : "v"(vo), "s"(sb) : "memory"); return r; }
DI u32x2 gld2(const void* sb, u32 vo) { u32x2 r; asm volatile("global_load_dwordx2 %0, %1, %2" : "=v"(r) : "v"(vo), "s"(sb) : "memory"); return r; }
DI u32 gld1c(const void* sb, u32 vo) { u32 r; asm volatile("global_load_dword %0, %1, %2 sc1" : "=v"(r) : "v"(vo), "s"(sb) : "memory"); return r; }
DI void gst4c(void* sb, u32 vo, u32x4 d) { asm volatile("global_store_dwordx4 %0, %1, %2 sc1\n\ts_nop 1" :: "v"(vo), "v"(d), "s"(sb) : "memory"); }
DI void gst2c(void* sb, u32 vo, u32x2 d) { asm volatile("global_store_dwordx2 %0, %1, %2 sc1\n\ts_nop 1" :: "v"(vo), "v"(d), "s"(sb) : "memory"); }
DI void gst2(void* sb, u32 vo, u32x2 d) { asm volatile("global_store_dwordx2 %0, %1, %2\n\ts_nop 1" :: "v"(vo), "v"(d), "s"(sb) : "memory"); }
DI void dma(const void* sb, u32 vo, u32 lds) {
    u32 keep;
    asm volatile("s_mov_b32 %0, m0\n\ts_mov_b32 m0, %3\n\ts_nop 0\n\tglobal_load_lds_dwordx4 %1, %2\n\ts_mov_b32 m0, %0" : "=&s"(keep) : "v"(vo), "s"(sb), "s"(lds) : "memory");
}
DI void dmac(const void* sb, u32 vo, u32 lds) {
    u32 keep;
    asm volatile("s_mov_b32 %0, m0\n\ts_mov_b32 m0, %3\n\ts_nop 0\n\tglobal_load_lds_dwordx4 %1, %2 sc1\n\ts_mov_b32 m0, %0" : "=&s"(keep) : "v"(vo), "s"(sb), "s"(lds) : "memory");
}
constexpr int NW2A = 14;
constexpr int RS_LD = 3, RS_CK = 6;
constexpr int PACE = 2;
template <int N> DI void wait_vm() { asm volatile("s_waitcnt vmcnt(%0)" :: "n"(N) : "memory"); }
#define PIN(x) asm volatile("" : "+v"(x))
DI void bar() { asm volatile("s_waitcnt lgkmcnt(0)\n\ts_barrier" ::: "memory"); }

template <int CTRL, int RM> DI float dpp_add(float v) {
    const int t = __builtin_amdgcn_update_dpp(0, __float_as_int(v), CTRL, RM, 0xf, true);
    return v + __int_as_float(t);
}
DI float wave_sum(float v) {
    v = dpp_add<0x111, 0xf>(v); v = dpp_add<0x112, 0xf>(v); v = dpp_add<0x114, 0xf>(v); v = dpp_add<0x118, 0xf>(v);
    v = dpp_add<0x142, 0xa>(v); v = dpp_add<0x143, 0xc>(v);
    return __int_as_float(__builtin_amdgcn_readlane(__float_as_int(v), 63));
}
DI s16x4 tr_read(const LAS char* p) { return __builtin_amdgcn_ds_read_tr16_b64_v4i16((LAS s16x4*)p); }
DI bf16x8 cat8(s16x4 a, s16x4 b) { return __builtin_shufflevector(a, b, 0, 1, 2, 3, 4, 5, 6, 7); }
DI float dot2(u32 a, u32 b, float c) { return __builtin_amdgcn_fdot2_f32_bf16(__builtin_bit_cast(bf16x2_t, a), __builtin_bit_cast(bf16x2_t, b), c, false); }

constexpr u64 SPIN_TICKS = 2000000;
DI void poll_tag(const char* base, u32 vo, u64 tag) {
    const u64 t0 = __builtin_amdgcn_s_memrealtime();
    for (;;) {
        u32x2 a;
        asm volatile("global_load_dwordx2 %0, %1, %2 sc1\n\ts_waitcnt vmcnt(0)" : "=&v"(a) : "v"(vo), "s"(base) : "memory");
        if (__all(a.x == (u32)tag && a.y == (u32)(tag >> 32))) break;
        if (__builtin_amdgcn_s_memrealtime() - t0 > SPIN_TICKS) break;
    }
}
constexpr int NSH = 16;
DI void arrive_all(char* shard_cnt, char* top_cnt, char* rel, u64 tag, int lane) {
    u32 old = 0;
    if (lane == 0) old = __hip_atomic_fetch_add((GAS u32*)shard_cnt, 1u, __ATOMIC_RELAXED, __HIP_MEMORY_SCOPE_AGENT);
    if (__builtin_amdgcn_readfirstlane(old) == (u32)(NWG / NSH - 1)) {
        u32 o2 = 0;
        if (lane == 0) o2 = __hip_atomic_fetch_add((GAS u32*)top_cnt, 1u, __ATOMIC_RELAXED, __HIP_MEMORY_SCOPE_AGENT);
        if (__builtin_amdgcn_readfirstlane(o2) == (u32)(NSH - 1)) { if (lane < 32) __hip_atomic_store((GAS u64*)(rel + lane * 256), tag, __ATOMIC_RELAXED, __HIP_MEMORY_SCOPE_AGENT); }
    }
}
DI void arrive_rel(char* cnt, u32 last, char* rel, u64 tag) {
    if (__hip_atomic_fetch_add((GAS u32*)cnt, 1u, __ATOMIC_RELAXED, __HIP_MEMORY_SCOPE_AGENT) == last) __hip_atomic_store((GAS u64*)rel, tag, __ATOMIC_RELAXED, __HIP_MEMORY_SCOPE_AGENT);
}
DI u32 swz(u32 row) { return ((row & 3) << 2) | ((row >> 2) & 3); }

__global__ void __launch_bounds__(NT, 2) fused_kernel(Params p) {
    extern __shared__ __attribute__((aligned(16))) char smem_[];
    LAS char* const L = (LAS char*)smem_;
    int tid = threadIdx.x, lane = tid & 63;
    const int w = __builtin_amdgcn_readfirstlane(tid >> 6);
    const int b = blockIdx.x, xg = b & 7, jb = b >> 3;
    u64 tag; asm volatile("s_load_dwordx2 %0, %1, 0x0" : "=s"(tag) : "s"(p.ws + WS_GEN) : "memory");

    const bool g1 = jb < 28;
    const int rt = 2 * xg + (jb >= 14 ? 1 : 0), ct = jb >= 28 ? 13 : jb >= 14 ? jb - 14 : jb;
    u32 prow = 4 * w + (lane >> 4);
    u32 pch = ((lane & 15) ^ swz(prow)) * 16;
    auto stage_w1p = [&](int kc, int slot, int half) {
        dma((const char*)p.w1 + (size_t)kc * 64 * FF * 2 + ct * 256, (prow + 32 * half) * (FF * 2) + pch, slot * 16384 + half * 8192 + w * 1024);
    };
    u32x2 kr[5], vr[2]; u32x4 skv[2], xrow, fhead;
    {
        const int rp = tid >> 4, c4 = tid & 15, s = 64 * jb + 2 * rp;
        const u32 co = (64 * xg + 4 * c4) * 2;
#pragma unroll
        for (int j = 0; j < 5; ++j) { const int r = s - 3 + j; kr[j] = gld2(p.k, (r < 0 ? 0 : r) * 1024 + co); }
        vr[0] = gld2(p.v, s * 1024 + co); vr[1] = gld2(p.v, s * 1024 + 1024 + co);
        skv[0] = gld4(p.sk, co * 4); skv[1] = gld4(p.sk, co * 4 + 16);
        dma(p.x, (64 * jb + 8 * w + (lane >> 3)) * 1024 + xg * 128 + (lane & 7) * 16, L_XI + w * 1024);
        xrow = gld4(p.x, (8 * b + w) * 1024 + lane * 16);
    }
    __builtin_amdgcn_s_barrier();
    const int ca = 64 * xg + 2 * jb;
    {
        const int cc = w >> 2, cp = w & 3;
        const u32 rowb = (u32)(ca + cc) * 4096;
        const u32 dst = L_F + cc * F_CH + (260 * cp + (cp > 0 ? 1 : 0)) * 16;
#pragma unroll
        for (int pc = 0; pc < 4; ++pc) {
            u32 o = 16 * (64 * pc + lane) + 4 * cp; if (o > 4080) o = 4080;
            dma(p.ssm, rowb + o, dst + 64 + pc * 1024);
        }
        fhead = gld4(p.ssm, rowb);
        if (tid < 112) { const int cpy = tid / 28, ch = tid - cpy * 28; *(LAS u32x4*)(L + L_X + cpy * X_ST + (256 + ch) * 16) = (u32x4){0, 0, 0, 0}; }
    }
    asm volatile("s_waitcnt lgkmcnt(0)" : "+s"(tag) :: "memory");
    tag += 1;
    static_assert(WS_R3 + 32 * 256 <= WS_KVR, "workspace map");
    if (b < NSH && w == 7) {
        if (lane < (b == 0 ? 2 : 1)) gst4c(p.ws + WS_C1, lane == 0 ? b * 256 : NSH * 256, (u32x4){(u32)tag, (u32)(tag >> 32), 0u, 0u});
        if (lane < (b == 0 ? 2 : 1) + 32 / NSH) gst2c(p.ws + WS_C2, lane < 32 / NSH ? 40 * 256 + (b * (32 / NSH) + lane) * 256 : lane == 32 / NSH ? b * 256 : NSH * 256, (u32x2){0u, 0u});
    }
    {
        const int rp = tid >> 4, c4 = tid & 15, s = 64 * jb + 2 * rp;
        asm volatile("s_waitcnt vmcnt(7)" : "+v"(kr[0]), "+v"(kr[1]), "+v"(kr[2]), "+v"(kr[3]), "+v"(kr[4]), "+v"(vr[0]), "+v"(vr[1]), "+v"(skv[0]), "+v"(skv[1]) :: "memory");
#pragma unroll
        for (int j = 0; j < 3; ++j) if (s - 3 + j < 0) kr[j] = (u32x2){0, 0};
        f32x2 kf[5][2], wf[4][2];
#pragma unroll
        for (int j = 0; j < 5; ++j) { kf[j][0] = (f32x2){bflo(kr[j].x), bfhi(kr[j].x)}; kf[j][1] = (f32x2){bflo(kr[j].y), bfhi(kr[j].y)}; }
#pragma unroll
        for (int q = 0; q < 2; ++q) {
            wf[0][q] = (f32x2){bflo(skv[q].x), bflo(skv[q].z)}; wf[1][q] = (f32x2){bfhi(skv[q].x), bfhi(skv[q].z)};
            wf[2][q] = (f32x2){bflo(skv[q].y), bflo(skv[q].w)}; wf[3][q] = (f32x2){bfhi(skv[q].y), bfhi(skv[q].w)};
        }
#pragma unroll
        for (int o = 0; o < 2; ++o) {
            u32x2 r;
#pragma unroll
            for (int q = 0; q < 2; ++q) {
                f32x2 a = kf[3 + o][q] * wf[0][q];
                a = kf[2 + o][q] * wf[1][q] + a; a = kf[1 + o][q] * wf[2][q] + a; a = kf[o][q] * wf[3][q] + a;
                const u32 vd = q ? vr[o].y : vr[o].x;
                a = a * (f32x2){bflo(vd), bfhi(vd)};
                if (q) r.y = pkbf(a.x, a.y); else r.x = pkbf(a.x, a.y);
            }
            *(LAS u32x2*)(L + L_KV + (2 * rp + o) * 128 + c4 * 8) = r;
        }
        asm volatile("s_waitcnt vmcnt(5)" : "+v"(xrow) :: "memory");
        bar();
        {
            const int g = lane >> 4, i = lane & 15, q = i >> 2, pp = lane & 3;
            const u32 ra = 8 * (2 * g + (pp >> 1)), cofs = (8 * w + 4 * (pp & 1)) * 2;
            const s16x4 a0 = tr_read(L + L_KV + (ra + 7 - q) * 128 + cofs), a1 = tr_read(L + L_KV + (ra + 3 - q) * 128 + cofs);
            const s16x4 x0 = tr_read(L + L_XI + (ra + q) * 128 + cofs), x1 = tr_read(L + L_XI + (ra + 4 + q) * 128 + cofs);
            const u32 c = 64 * xg + 8 * w + (i & 7), rc = 2 * g + (i >> 3);
            gst4c(p.ws + WS_KVR, c * 4096 + (2040 - 64 * jb - 8 * rc) * 2, __builtin_bit_cast(u32x4, cat8(a0, a1)));
            gst4c(p.ws + WS_XT, c * 4096 + (64 * jb + 8 * rc) * 2, __builtin_bit_cast(u32x4, cat8(x0, x1)));
        }
        float s1 = 0.f, s2 = 0.f;
#pragma unroll
        for (int i = 0; i < 4; ++i) { const float a = bflo(xrow[i]), c = bfhi(xrow[i]); s1 += a + c; s2 += a * a + c * c; }
        s1 = wave_sum(s1); s2 = wave_sum(s2);
        const float mean = s1 * (1.0f / DM);
        const float var = fmaxf(s2 * (1.0f / DM) - mean * mean, 0.f);
        const float rstd = __builtin_amdgcn_rsqf(var + 1e-5f);
        if (lane == 0) asm volatile("global_store_dword %0, %1, %2 sc1" :: "v"((w >> 2) * 4096 + (255 - b) * 16 + (w & 3) * 4), "v"(pkbf(mean, rstd)), "s"(p.ws + WS_STATS) : "memory");
    }
    {
        asm volatile("s_waitcnt vmcnt(3)" : "+v"(fhead) :: "memory");
        const int cc = w >> 2, cp = w & 3;
        if (lane < 4) {
            u32x4 h = {0, 0, 0, 0};
            if (lane == 3) {
                h.w = cp >= 1 ? (cp == 1 ? fhead.x : cp == 2 ? fhead.y : fhead.z) : 0u;
                h.z = cp >= 2 ? (cp == 2 ? fhead.x : fhead.y) : 0u;
                h.y = cp >= 3 ? fhead.x : 0u;
            }
            *(LAS u32x4*)(L + L_F + cc * F_CH + (260 * cp + (cp > 0 ? 1 : 0)) * 16 + lane * 16) = h;
        }
    }
    if (w == 0) poll_tag(p.ws + WS_C1, (lane & 1) ? NSH * 256 : (b & (NSH - 1)) * 256, tag);
    wait_vm<0>();
    bar();
    if (w == 0) arrive_all(p.ws + WS_C1 + (b & (NSH - 1)) * 256 + 8, p.ws + WS_C1 + NSH * 256 + 8, p.ws + WS_R1, tag, lane);
    const int w2blk = 7 * b + (w > 0 ? w - 1 : 0);
    if (w > 0) {
        const char* g = (const char*)p.w1 + ct * 256;
#pragma unroll
        for (int i = 0; i < 10; ++i) {
            const int pc = w - 1 + 7 * i;
            if (pc < 64) { const u32 row = 4 * pc + (lane >> 4); dma(g, row * (FF * 2) + ((lane & 15) ^ swz(row)) * 16, pc * 1024); }
            __builtin_amdgcn_s_sleep(PACE);
        }
        dma(p.w2, ((w2blk >> 4) * 16 + (lane >> 2)) * 1024 + (w2blk & 15) * 64 + (lane & 3) * 16, L_W2S + (w - 1) * 1024);
        wait_vm<0>();
    }
    int h5 = lane >> 5, blk = (lane >> 4) & 1, tq = (lane & 15) >> 2, tp = lane & 3;
    auto relane = [&]() { asm volatile("" : "+v"(tid)); lane = tid & 63; prow = 4 * w + (lane >> 4); pch = ((lane & 15) ^ swz(prow)) * 16; h5 = lane >> 5; blk = (lane >> 4) & 1; tq = (lane & 15) >> 2; tp = lane & 3; };
    const int fq = w & 3, rh = w >> 2;
    u32 ao[2];
#pragma unroll
    for (int t = 0; t < 2; ++t) { const u32 row = 8 * h5 + 4 * t + tq; ao[t] = 256 * row + 16 * ((4 * fq + 2 * blk + (tp >> 1)) ^ swz(row)) + 8 * (tp & 1); }
    bf16x8 wa[32];
    {
        if (w == 0) poll_tag(p.ws + WS_R1, jb * 256, tag);
        bar();
        const int cc = tid >> 8, j8 = tid & 255;
        const u32 crow = (u32)(ca + cc) * 4096;
        const int s0 = 2040 - 8 * j8;
        u32x4 kvc = gld4c(p.ws + WS_KVR, crow + j8 * 16);
        u32 kvn = gld1c(p.ws + WS_KVR, crow + (j8 < 255 ? j8 * 16 + 16 : 0));
        u32x4 xc = gld4c(p.ws + WS_XT, crow + s0 * 2);
        u32x4 st[2];
#pragma unroll
        for (int i = 0; i < 2; ++i) st[i] = gld4c(p.ws + WS_STATS, i * 4096 + j8 * 16);
        u32 sdv; asm volatile("global_load_ushort %0, %1, %2" : "=v"(sdv) : "v"((ca + cc) * 2), "s"(p.sD) : "memory");
#pragma unroll
        for (int s = 0; s < 16; ++s) wa[s] = cat8(tr_read(L + s * 4096 + ao[0]), tr_read(L + s * 4096 + ao[1]));
        const int cm = lane & 15, cg = lane >> 4, ccp = (cm + 1) & 3;
        const LAS char* ab = L + L_F + (w >> 2) * F_CH + (260 * ccp + (ccp > 0 ? 1 : 0)) * 16 + 16 * cg + 16 * ((cm + 1) >> 2) + 256 * ((w >> 1) & 1);
        bf16x8 A0[8];
#pragma unroll
        for (int i = 0; i < 8; ++i) A0[i] = *(const LAS bf16x8*)(ab + 512 * i);
        asm volatile("s_waitcnt vmcnt(4)" : "+v"(kvc), "+v"(kvn) :: "memory");
        if (j8 == 255) kvn = 0;
        {
            u32x4 sh;
            sh.x = __builtin_amdgcn_alignbit(kvc.y, kvc.x, 16); sh.y = __builtin_amdgcn_alignbit(kvc.z, kvc.y, 16);
            sh.z = __builtin_amdgcn_alignbit(kvc.w, kvc.z, 16); sh.w = __builtin_amdgcn_alignbit(kvn, kvc.w, 16);
            *(LAS u32x4*)(L + L_X + (2 * cc + 1) * X_ST + j8 * 16) = kvc;
            *(LAS u32x4*)(L + L_X + (2 * cc) * X_ST + j8 * 16) = sh;
        }
        bar();
        {
            const int wc = w >> 2, kh = (w >> 1) & 1, ts = w & 1;
            const int m = lane & 15, g = lane >> 4;
            const LAS char* xb = L + L_X + (2 * wc + (m & 1)) * X_ST + 448 - 64 * (m >> 1) + 16 * g + 256 * kh;
            f32x4 acc[4];
#pragma unroll
            for (int i = 0; i < 4; ++i) acc[i] = (f32x4){0.f, 0.f, 0.f, 0.f};
#define MF16(acc, a, b) acc = __builtin_amdgcn_mfma_f32_16x16x32_bf16(a, b, acc, 0, 0, 0)
            if (ts == 0) {
                bf16x8 A[2][8], G[2][8];
#pragma unroll
                for (int i = 0; i < 8; ++i) { A[0][i] = A0[i]; G[0][7 - i] = *(const LAS bf16x8*)(xb + 512 * i); }
#pragma unroll
                for (int tt = 0; tt < 4; ++tt) {
                    const int c = tt & 1, n = c ^ 1;
#pragma unroll
                    for (int i = 0; i < 8; ++i) {
                        if (tt < 2 && (i & 1)) stage_w1p(4 + 2 * tt + (i >> 2), 2 * tt + (i >> 2), (i >> 1) & 1);
                        if (tt < 3) { A[n][i] = *(const LAS bf16x8*)(ab + 512 * i + 64 * (tt + 1)); G[n][7 - i] = *(const LAS bf16x8*)(xb + 512 * i + 64 * (tt + 1)); }
                        MF16(acc[0], A[c][i], G[c][7 - i]);
                        if (i < 7) MF16(acc[1], A[c][i], G[c][6 - i]);
                        if (i < 2) MF16(acc[2], A[c][i], G[c][1 - i]);
                        if (i < 1) MF16(acc[3], A[c][i], G[c][0]);
                        __builtin_amdgcn_sched_barrier(0);
                    }
                }
            } else {
                bf16x8 A[2][6], G[2][6];
#pragma unroll
                for (int i = 0; i < 6; ++i) { A[0][i] = A0[i]; G[0][5 - i] = *(const LAS bf16x8*)(xb + 512 * (i + 2)); }
#pragma unroll
                for (int tt = 0; tt < 4; ++tt) {
                    const int c = tt & 1, n = c ^ 1;
#pragma unroll
                    for (int i = 0; i < 6; ++i) {
                        if (tt < 2 && i < 4) stage_w1p(4 + 2 * tt + (i >> 1), 2 * tt + (i >> 1), i & 1);
                        if (tt < 3) { A[n][i] = *(const LAS bf16x8*)(ab + 512 * i + 64 * (tt + 1)); G[n][5 - i] = *(const LAS bf16x8*)(xb + 512 * (i + 2) + 64 * (tt + 1)); }
                        MF16(acc[0], A[c][i], G[c][5 - i]);
                        if (i < 5) MF16(acc[1], A[c][i], G[c][4 - i]);
                        if (i < 4) MF16(acc[2], A[c][i], G[c][3 - i]);
                        if (i < 3) MF16(acc[3], A[c][i], G[c][2 - i]);
                        __builtin_amdgcn_sched_barrier(0);
                    }
                }
            }
            LAS float* pb = (LAS float*)(L + L_P + (2 * kh + wc) * P_ST) + 36 * (m >> 1) + 8 * g + (m & 1);
#pragma unroll
            for (int i = 0; i < 4; ++i) {
                const int q = ts == 0 ? (i == 0 ? 7 : i == 1 ? 6 : i == 2 ? 1 : 0) : 5 - i;
#pragma unroll
                for (int r = 0; r < 4; ++r) pb[36 * 8 * q + 2 * r] = acc[i][r];
            }
        }
        asm volatile("s_waitcnt vmcnt(0)" : "+v"(xc), "+v"(st[0]), "+v"(st[1]), "+v"(sdv) :: "memory");
        bar();
        {
            const LAS char* p0 = L + L_P + cc * P_ST + (36 * (s0 >> 5) + (s0 & 31)) * 4;
            const f32x4 ya = *(const LAS f32x4*)p0, yb = *(const LAS f32x4*)(p0 + 16);
            const f32x4 yc = *(const LAS f32x4*)(p0 + 2 * P_ST), yd = *(const LAS f32x4*)(p0 + 2 * P_ST + 16);
            const float dd = bflo(sdv);
            float h[8];
#pragma unroll
            for (int e = 0; e < 8; ++e) {
                const float y = (e < 4 ? ya[e & 3] + yc[e & 3] : yb[e & 3] + yd[e & 3]);
                const u32 kd = kvc[(7 - e) >> 1], xd = xc[e >> 1];
                const float kv = (e & 1) ? bflo(kd) : bfhi(kd);
                const float x = (e & 1) ? bfhi(xd) : bflo(xd);
                const float mean = bflo(st[e >> 2][e & 3]), rstd = bfhi(st[e >> 2][e & 3]);
                const float y2 = y + kv * dd;
                h[e] = x + (x - mean) * rstd * y2;
            }
            gst4c(p.ws + WS_HT, (u32)(s0 >> 7) * 131072 + (u32)(ca + cc) * 256 + (s0 & 127) * 2, (u32x4){pkbf(h[0], h[1]), pkbf(h[2], h[3]), pkbf(h[4], h[5]), pkbf(h[6], h[7])});
        }
        if (w > 0) {
            const LAS char* q = L + L_W2S + (w - 1) * 1024 + (4 * h5 + tq) * 64 + blk * 32 + tp * 8;
            gst4c(p.ws + WS_W2T, w2blk * 1024 + lane * 16, __builtin_bit_cast(u32x4, cat8(tr_read(q), tr_read(q + 512))));
        }
#pragma unroll
        for (int s = 16; s < 32; ++s) wa[s] = cat8(tr_read(L + (s - 16) * 4096 + ao[0]), tr_read(L + (s - 16) * 4096 + ao[1]));
        wait_vm<0>();
        bar();
        if (w == 0) arrive_all(p.ws + WS_C2 + (b & (NSH - 1)) * 256, p.ws + WS_C2 + NSH * 256, p.ws + WS_R2, tag, lane);
        {
            float cs = 0.f, cs2 = 0.f;
            if (rh == 0) {
#pragma unroll
                for (int s = 0; s < 16; ++s) { const u32x4 u = __builtin_bit_cast(u32x4, wa[s]); cs = dot2(u.x, 0x3f803f80u, cs); cs2 = dot2(u.y, 0x3f803f80u, cs2); cs = dot2(u.z, 0x3f803f80u, cs); cs2 = dot2(u.w, 0x3f803f80u, cs2); }
            } else {
#pragma unroll
                for (int s = 16; s < 32; ++s) { const u32x4 u = __builtin_bit_cast(u32x4, wa[s]); cs = dot2(u.x, 0x3f803f80u, cs); cs2 = dot2(u.y, 0x3f803f80u, cs2); cs = dot2(u.z, 0x3f803f80u, cs); cs2 = dot2(u.w, 0x3f803f80u, cs2); }
            }
            cs += cs2;
            const auto r = __builtin_amdgcn_permlane32_swap(__float_as_uint(cs), __float_as_uint(cs), false, false);
            cs = __uint_as_float(r[0]) + __uint_as_float(r[1]);
            if (lane < 32) ((LAS float*)(L + L_TAB + 1024))[rh * 128 + 32 * fq + lane] = cs;
        }
    }

    relane();
    const int rt2 = 4 * xg + (jb >> 3), dt = jb & 7, kq = w >> 1, dh = w & 1;
    const char* w2t = p.ws + WS_W2T + (size_t)(28 * kq * 16 + 2 * dt + dh) * 1024;
    u32x4 wf[28];
    auto load_w2 = [&](int i) { wf[i] = gld4c(w2t + (size_t)i * 16384, lane * 16); };
    {
        LAS f32x4* TB = (LAS f32x4*)(L + L_TAB);
        u32 b1v; asm volatile("global_load_dword %0, %1, %2" : "=v"(b1v) : "v"((ct * 128 + 2 * (tid & 63)) * 2), "s"(p.b1) : "memory");
        if (w == 0) poll_tag(p.ws + WS_R2, jb * 256, tag);
        asm volatile("s_waitcnt vmcnt(0)" : "+v"(b1v) :: "memory");
        bar();
        if (g1) {
            const char* hg = p.ws + WS_HT + (size_t)rt * 131072;
            auto stage_hp = [&](int j) { dmac(hg + j * 8192, prow * 256 + pch, j * 8192 + w * 1024); };
            stage_hp(0); stage_hp(1);
            __builtin_amdgcn_s_barrier();
#pragma unroll
            for (int j = 2; j < 8; ++j) stage_hp(j);
            const LAS char* hb[2][4];
#pragma unroll
            for (int i = 0; i < 4; ++i) {
                const u32 row = 8 * h5 + 4 * (i & 1) + tq;
                const u32 o = 256 * row + 16 * ((4 * (2 * rh + (i >> 1)) + 2 * blk + (tp >> 1)) ^ swz(row)) + 8 * (tp & 1);
#pragma unroll
                for (int r = 0; r < 2; ++r) { u32 oo = o + r * 65536; asm volatile("" : "+v"(oo)); hb[r][i] = L + oo; }
            }
            f32x16 acA, acB;
#pragma unroll
            for (int i = 0; i < 16; ++i) { acA[i] = 0.f; acB[i] = 0.f; }
            bf16x8 fb[3][2];
            auto ldu = [&](int u) {
                const int t = u >> 4;
#pragma unroll
                for (int e = 0; e < 2; ++e) { const int s = 2 * (u & 15) + e; fb[u % 3][e] = cat8(tr_read(hb[s >> 4][2 * t] + (s & 15) * 4096), tr_read(hb[s >> 4][2 * t + 1] + (s & 15) * 4096)); }
            };
            const u32 tl = (u32)tag, th = (u32)(tag >> 32);
            if (tid == 64) *(LAS u32*)(L + L_TAB + 2048) = 0u;
            if (tid < 64) {
                const LAS f32x2* cp = (const LAS f32x2*)(L + L_TAB + 1024);
                const f32x2 c = cp[tid] + cp[64 + tid];
                TB[tid] = (f32x4){c.x, bflo(b1v), c.y, bfhi(b1v)};
            }
            wait_vm<6>();
            bar();
            ldu(0); ldu(1);
            static_for<16>([&](auto U_) {
                constexpr int u = decltype(U_)::value;
                acA = __builtin_amdgcn_mfma_f32_32x32x16_bf16(wa[2 * u], fb[u % 3][0], acA, 0, 0, 0);
                acB = __builtin_amdgcn_mfma_f32_32x32x16_bf16(wa[2 * u + 1], fb[u % 3][1], acB, 0, 0, 0);
                __builtin_amdgcn_sched_barrier(0);
                if constexpr ((u & 1) == 0 && u < 14) {
                    constexpr int kc = (u + 2) >> 1;
                    wait_vm<(kc == 1 ? 4 : kc == 2 ? 6 : kc == 3 ? 8 : 2 * (7 - kc))>();
                    bar();
                }
                if constexpr (u < 4) { stage_hp(8 + 2 * u); stage_hp(9 + 2 * u); }
                ldu(u + 2);
                __builtin_amdgcn_sched_barrier(0);
            });
            f32x16 acc0 = acA + acB;
#pragma unroll
            for (int i = 0; i < 16; ++i) { acA[i] = 0.f; acB[i] = 0.f; }
            u32x4 rsg[2];
            float rr[2], rm[2];
            auto gelu2 = [&](const f32x16& acc, int a, int j) {
                const f32x4 tt = TB[16 * fq + 4 * (j >> 1) + 2 * h5 + (j & 1)];
                const f32x2 c2 = {tt[0], tt[2]}, b2 = {tt[1], tt[3]}, a2 = {acc[2 * j], acc[2 * j + 1]};
                const f32x2 u = a2 * rr[a] + (c2 * rm[a] + b2);
                const f32x2 z = u * (u * u * (-0.1029432f) + (-2.3022082f));
                f32x2 d; d.x = __builtin_amdgcn_exp2f(z.x); d.y = __builtin_amdgcn_exp2f(z.y);
                d = d + 1.0f;
                f32x2 r; r.x = __builtin_amdgcn_rcpf(d.x); r.y = __builtin_amdgcn_rcpf(d.y);
                r = r * u;
                return pkbf(r.x, r.y);
            };
            const u32 hoffs = ((u32)(4 * rt + 2 * rh) * 112 + (128 * ct + 32 * fq) / 16) * 1024 + lane * 16;
            const u32 rso = (128 * rt + 64 * rh + (lane & 31)) * 16;
            u32 pk[8];
            static_for<16>([&](auto U_) {
                constexpr int u = decltype(U_)::value;
                if constexpr (u == RS_CK) {
                    asm volatile("s_waitcnt vmcnt(%2)" : "+v"(rsg[0]), "+v"(rsg[1]) : "n"(RS_CK > 16 - NW2A ? RS_CK - (RS_LD > 16 - NW2A ? RS_LD : 16 - NW2A) : 0) : "memory");
                    if (!__all(rsg[0].z == tl && rsg[0].w == th && rsg[1].z == tl && rsg[1].w == th)) {
                        const u64 t0 = __builtin_amdgcn_s_memrealtime();
                        do {
                            __builtin_amdgcn_s_sleep(2);
                            asm volatile("global_load_dwordx4 %0, %2, %3 sc1\n\tglobal_load_dwordx4 %1, %2, %3 offset:512 sc1\n\ts_waitcnt vmcnt(0)" : "=&v"(rsg[0]), "=&v"(rsg[1]) : "v"(rso), "s"(p.ws + WS_RS) : "memory");
                        } while (!__all(rsg[0].z == tl && rsg[0].w == th && rsg[1].z == tl && rsg[1].w == th) && __builtin_amdgcn_s_memrealtime() - t0 < SPIN_TICKS);
                    }
                    rr[0] = __uint_as_float(rsg[0].x); rm[0] = __uint_as_float(rsg[0].y); rr[1] = __uint_as_float(rsg[1].x); rm[1] = __uint_as_float(rsg[1].y);
                }
                acA = __builtin_amdgcn_mfma_f32_32x32x16_bf16(wa[2 * u], fb[(u + 16) % 3][0], acA, 0, 0, 0);
                acB = __builtin_amdgcn_mfma_f32_32x32x16_bf16(wa[2 * u + 1], fb[(u + 16) % 3][1], acB, 0, 0, 0);
                if constexpr (u >= RS_CK && u < RS_CK + 8) { pk[u - RS_CK] = gelu2(acc0, 0, u - RS_CK); PIN(pk[u - RS_CK]); }
                if constexpr (u == RS_CK + 3) gst4c(p.ws + WS_H, hoffs, (u32x4){pk[0], pk[1], pk[2], pk[3]});
                if constexpr (u == RS_CK + 7) gst4c(p.ws + WS_H, hoffs + 1024, (u32x4){pk[4], pk[5], pk[6], pk[7]});
                __builtin_amdgcn_sched_barrier(0);
                if constexpr (u == RS_LD) { rsg[0] = gld4c(p.ws + WS_RS, rso); rsg[1] = gld4c(p.ws + WS_RS, rso + 512); }
                if constexpr (u >= 16 - NW2A) load_w2(u - (16 - NW2A));
                if constexpr (u < 14) ldu(u + 18);
                __builtin_amdgcn_sched_barrier(0);
            });
            const f32x16 acc1 = acA + acB;
#pragma unroll
            for (int j = 0; j < 4; ++j) { pk[j] = gelu2(acc1, 1, j); PIN(pk[j]); }
            wait_vm<(RS_CK + 7 > 16 - NW2A ? 9 - RS_CK : NW2A)>();
            u32 lastw = 0;
            if (lane == 0) lastw = __hip_atomic_fetch_add((LAS u32*)(L + L_TAB + 2048), 1u, __ATOMIC_RELAXED, __HIP_MEMORY_SCOPE_WORKGROUP) == 7u;
            lastw = __builtin_amdgcn_readfirstlane(lastw);
            u32 old3 = 0; u64 ex;
            asm volatile("s_cmp_eq_u32 %2, 0\n\ts_cbranch_scc1 .Lnoarr%=\n\ts_mov_b64 %1, exec\n\ts_mov_b64 exec, 1\n\tglobal_atomic_add %0, %3, %4, %5 sc0\n\ts_mov_b64 exec, %1\n.Lnoarr%=:"
                         : "+v"(old3), "=&s"(ex) : "s"(lastw), "v"(0), "v"(1u), "s"(p.ws + WS_C3 + rt * 256) : "scc", "memory");
            gst4c(p.ws + WS_H, hoffs + 112 * 1024, (u32x4){pk[0], pk[1], pk[2], pk[3]});
#pragma unroll
            for (int j = 4; j < 8; ++j) { pk[j] = gelu2(acc1, 1, j); PIN(pk[j]); }
            gst4c(p.ws + WS_H, hoffs + 112 * 1024 + 1024, (u32x4){pk[4], pk[5], pk[6], pk[7]});
            asm volatile("s_waitcnt vmcnt(2)" : "+v"(old3) :: "memory");
            if (lastw && __builtin_amdgcn_readfirstlane(old3) == 13u) { if (lane == 0) __hip_atomic_store((GAS u64*)(p.ws + WS_R3 + rt * 256), tag, __ATOMIC_RELAXED, __HIP_MEMORY_SCOPE_AGENT); }
            wait_vm<2>();
#pragma unroll
            for (int i = 0; i < NW2A; ++i) PIN(wf[i]);
        } else {
            const int idx = jb - 28, srt = 2 * xg + (idx >> 1), ta = w & 1;
            const char* g = p.ws + WS_HT + (size_t)srt * 131072 + (idx & 1) * 128;
#pragma unroll
            for (int i = 0; i < 8; ++i) dmac(g, (128 * kq + 16 * i + (lane >> 2)) * 256 + ta * 64 + (lane & 3) * 16, w * 8192 + i * 1024);
            f32x16 gs, gq, gs2, gq2;
#pragma unroll
            for (int i = 0; i < 16; ++i) { gs[i] = 0.f; gq[i] = 0.f; gs2[i] = 0.f; gq2[i] = 0.f; }
            bf16x8 ones;
#pragma unroll
            for (int i = 0; i < 8; ++i) ones[i] = (short)0x3f80;
            const LAS char* fp = L + w * 8192 + (8 * h5 + tq) * 64 + blk * 32 + tp * 8;
            static_for<4>([&](auto S_) {
                constexpr int s = 2 * decltype(S_)::value;
                wait_vm<6 - s>();
                const bf16x8 f0 = cat8(tr_read(fp + s * 1024), tr_read(fp + s * 1024 + 256)), f1 = cat8(tr_read(fp + s * 1024 + 1024), tr_read(fp + s * 1024 + 1280));
                gq = __builtin_amdgcn_mfma_f32_32x32x16_bf16(f0, f0, gq, 0, 0, 0);
                gs = __builtin_amdgcn_mfma_f32_32x32x16_bf16(ones, f0, gs, 0, 0, 0);
                gq2 = __builtin_amdgcn_mfma_f32_32x32x16_bf16(f1, f1, gq2, 0, 0, 0);
                gs2 = __builtin_amdgcn_mfma_f32_32x32x16_bf16(ones, f1, gs2, 0, 0, 0);
            });
            gq += gq2; gs[0] += gs2[0];
            {
                const int c = lane & 31, reg = (c & 3) + 4 * (c >> 3);
                float q = 0.f;
#pragma unroll
                for (int i = 0; i < 16; ++i) q = reg == i ? gq[i] : q;
                if (((c >> 2) & 1) == h5) *(LAS f32x2*)(L + 65536 + ((kq * 64) + 32 * ta + c) * 8) = (f32x2){gs[0], q};
            }
            bar();
            if (tid < 64) {
                f32x2 t = {0.f, 0.f};
#pragma unroll
                for (int i = 0; i < 4; ++i) t += *(const LAS f32x2*)(L + 65536 + (i * 64 + tid) * 8);
                const float mean = t.x * (1.0f / DM);
                const float var = fmaxf(t.y * (1.0f / DM) - mean * mean, 0.f);
                const float r1 = __builtin_amdgcn_rsqf(var + 1e-5f);
                const float r2 = __builtin_amdgcn_rsqf(var * r1 * r1 + 1e-5f);
                const float rr = r1 * r2;
                gst4c(p.ws + WS_RS, (128 * srt + 64 * (idx & 1) + tid) * 16, (u32x4){__float_as_uint(rr), __float_as_uint(-rr * mean), (u32)tag, (u32)(tag >> 32)});
            }
#pragma unroll
            for (int i = 0; i < NW2A; ++i) load_w2(i);
            wait_vm<0>();
#pragma unroll
            for (int i = 0; i < NW2A; ++i) PIN(wf[i]);
        }
        relane();
        const char* c3 = p.ws + WS_R3 + (rt2 >> 1) * 256;
        u32x2 s3a;
        asm volatile("s_cmp_lg_u32 %1, 0\n\ts_cbranch_scc1 .Lskipa%=\n\tglobal_load_dwordx2 %0, %2, %3 sc1\n.Lskipa%=:" : "=&v"(s3a) : "s"(w), "v"(0), "s"(c3) : "scc", "memory");
#pragma unroll
        for (int i = NW2A; i < 28; ++i) load_w2(i);
        asm volatile("s_waitcnt vmcnt(%1)" : "+v"(s3a) : "n"(28 - NW2A) : "memory");
        if (w == 0) { if (!__all(s3a.x == (u32)tag && s3a.y == (u32)(tag >> 32))) poll_tag(c3, 0, tag); }
        bar();
        if (g1 && tid == 0) arrive_rel(p.ws + WS_C3 + (16 + rt) * 256, 13u, p.ws + WS_R3 + (16 + rt) * 256, tag);

        u32x2 bv = gld2(p.b2, (64 * dt + 32 * dh + 8 * kq + 4 * h5) * 2);
        const char* hb = p.ws + WS_H + ((size_t)(2 * rt2) * 112 + 28 * kq + dh) * 1024;
        u32 oa = kq * 28 * 1024 + lane * 16;
        asm volatile("" : "+v"(oa));
        u32x2 s3b;
        f32x16 acc;
        auto put_partials = [&](u32 base) {
#pragma unroll
            for (int g = 0; g < 4; ++g)
                if (g != kq) *(LAS f32x4*)(L + base + ((g * 4 + kq) * 2 + dh) * 1024 + lane * 16) = (f32x4){acc[4 * g], acc[4 * g + 1], acc[4 * g + 2], acc[4 * g + 3]};
        };
        f32x4 own;
        auto keep_own = [&]() {
            if (kq == 0) own = (f32x4){acc[0], acc[1], acc[2], acc[3]};
            else if (kq == 1) own = (f32x4){acc[4], acc[5], acc[6], acc[7]};
            else if (kq == 2) own = (f32x4){acc[8], acc[9], acc[10], acc[11]};
            else own = (f32x4){acc[12], acc[13], acc[14], acc[15]};
        };
        auto finish = [&](int a, u32 base) {
            const int d0 = 32 * dh + 8 * kq + 4 * h5;
            f32x4 s = own;
#pragma unroll
            for (int o = 1; o < 4; ++o) { const int src = (kq + o) & 3; s += *(const LAS f32x4*)(L + base + ((kq * 4 + src) * 2 + dh) * 1024 + lane * 16); }
            const s16x4 hv = tr_read(L + L_HT2 + (32 * dh + 8 * kq + 4 * h5 + tq) * 128 + (32 * a + 16 * blk + 4 * tp) * 2);
            const u32x2 hu = __builtin_bit_cast(u32x2, hv);
            const float o0 = s[0] + bflo(bv.x) + bflo(hu.x), o1 = s[1] + bfhi(bv.x) + bfhi(hu.x), o2 = s[2] + bflo(bv.y) + bflo(hu.y), o3 = s[3] + bfhi(bv.y) + bfhi(hu.y);
            gst2(p.out, (u32)(64 * rt2 + 32 * a + (lane & 31)) * 1024 + (64 * dt + d0) * 2, (u32x2){pkbf(o0, o1), pkbf(o2, o3)});
        };
        static_for<2>([&](auto A_) {
            constexpr int a = decltype(A_)::value;
            auto stage = [&](int j) { dmac(hb + a * 112 * 1024 + (size_t)j * 2048, lane * 16, (kq * 28 + 2 * j + dh) * 1024); };
            if constexpr (a == 1) {
                asm volatile("s_waitcnt vmcnt(0)" : "+v"(s3b) :: "memory");
                if (w == 0) { if (!__all(s3b.x == (u32)tag && s3b.y == (u32)(tag >> 32))) poll_tag(c3 + 16 * 256, 0, tag); }
                bar();
            }
            stage(0); stage(1);
            __builtin_amdgcn_s_barrier();
#pragma unroll
            for (int j = 2; j < 8; ++j) stage(j);
            if constexpr (a == 1) finish(0, L_R0);
            f32x16 acA, acB;
#pragma unroll
            for (int i = 0; i < 16; ++i) { acA[i] = 0.f; acB[i] = 0.f; }
            bf16x8 fb[3][2];
            auto ldu = [&](int t) { fb[t % 3][0] = *(const LAS bf16x8*)(L + oa + 2 * t * 1024); fb[t % 3][1] = *(const LAS bf16x8*)(L + oa + (2 * t + 1) * 1024); };
            wait_vm<6 + a>();
            if constexpr (a == 0) {
#pragma unroll
                for (int i = NW2A; i < 28; ++i) PIN(wf[i]);
                PIN(bv);
            }
            bar();
            ldu(0); ldu(1);
            static_for<14>([&](auto T_) {
                constexpr int t = decltype(T_)::value;
                acA = __builtin_amdgcn_mfma_f32_32x32x16_bf16(__builtin_bit_cast(bf16x8, wf[2 * t]), fb[t % 3][0], acA, 0, 0, 0);
                acB = __builtin_amdgcn_mfma_f32_32x32x16_bf16(__builtin_bit_cast(bf16x8, wf[2 * t + 1]), fb[t % 3][1], acB, 0, 0, 0);
                __builtin_amdgcn_sched_barrier(0);
                if constexpr ((t & 1) == 0 && t <= 10) {
                    wait_vm<(t == 10 ? 0 : t == 8 ? 2 : 4 + (a == 1 && t <= 4 ? 1 : 0))>();
                    bar();
                }
                if constexpr (t < 6) stage(8 + t);
                if constexpr (a == 0 && t == 10) asm volatile("s_cmp_lg_u32 %1, 0\n\ts_cbranch_scc1 .Lskipb%=\n\tglobal_load_dwordx2 %0, %2, %3 sc1\n.Lskipb%=:" : "=&v"(s3b) : "s"(w), "v"(0), "s"(c3 + 16 * 256) : "scc", "memory");
                if constexpr (a == 0 && t == 11) dmac(p.ws + WS_HT, (u32)(rt2 >> 1) * 131072 + (64 * dt + 8 * w + (lane >> 3)) * 256 + (rt2 & 1) * 128 + (lane & 7) * 16, L_HT2 + w * 1024);
                if constexpr (t < 12) ldu(t + 2);
                __builtin_amdgcn_sched_barrier(0);
            });
            acc = acA + acB;
            if constexpr (a == 0) { put_partials(L_R0); keep_own(); }
        });
        put_partials(L_R0); keep_own();
        bar();
        finish(1, L_R0);
    }
    if (b == 0 && tid == 0) __hip_atomic_store((GAS u64*)(p.ws + WS_GEN), tag, __ATOMIC_RELAXED, __HIP_MEMORY_SCOPE_AGENT);
}

extern "C" void kernel_launch(void* const* d_in, const int* in_sizes, int n_in, void* d_out, int out_size, void* d_ws, size_t ws_size, hipStream_t stream) {
    Params p;
    p.x = (const u16*)d_in[0]; p.k = (const u16*)d_in[1]; p.v = (const u16*)d_in[2]; p.sk = (const u16*)d_in[3]; p.ssm = (const u16*)d_in[4]; p.sD = (const u16*)d_in[5];
    p.w1 = (const u16*)d_in[7]; p.b1 = (const u16*)d_in[8]; p.w2 = (const u16*)d_in[9]; p.b2 = (const u16*)d_in[10];
    p.out = (u16*)d_out;
    p.ws = (char*)d_ws;
    (void)hipFuncSetAttribute((const void*)fused_kernel, hipFuncAttributeMaxDynamicSharedMemorySize, LDS_BYTES);
    fused_kernel<<<NWG, NT, LDS_BYTES, stream>>>(p);
}
```
